```python
import torch

def _softmax(x, axis=-1):
    return torch.softmax(x, dim=axis)
def _gelu(x):
    return torch.nn.functional.gelu(x, approximate="tanh")
def _silu(x):
    return torch.nn.functional.silu(x)
def _sigmoid(x):
    return torch.sigmoid(x)
def _layernorm(x, axis=-1, eps=1e-5):
    m = x.mean(dim=axis, keepdim=True)
    v = x.var(dim=axis, keepdim=True, unbiased=False)
    return (x - m) / torch.sqrt(v + eps)
def _scatter_add(tgt, idx, src, axis=0):
    return tgt.clone().index_add_(axis, idx, src)
def _segment_sum(src, seg, n):
    out = torch.zeros((n,) + src.shape[1:], dtype=src.dtype)
    return out.index_add_(0, seg, src)
def _pad(x, pw):
    flat = tuple(v for pair in reversed(pw) for v in pair)
    return torch.nn.functional.pad(x, flat)

kernel_name = 'recurrent_scan__sliding_block_cached__geglu_ffn__decode'

def setup_inputs():

    _w = (lambda a: (lambda t: t.to(torch.bfloat16) if t.is_floating_point() else t)(torch.as_tensor(a)))
    def _A_setup():
        B, Sq, D = 1, 1, 512
        Hq, Hkv, Sk, Dh = 32, 32, 32768, 128
        assert Hq % Hkv == 0, (Hq, Hkv)
        return {
            "x": _w((lambda *s: torch.randn(*s, dtype=torch.bfloat16) * 0.05)(B, Sq, D)),
            "k_cache": _w((lambda *s: torch.randn(*s, dtype=torch.bfloat16) * 1.6)(B, Hkv, Sk, Dh)),
            "v_cache": _w((lambda *s: torch.randn(*s, dtype=torch.bfloat16) * 0.2)(B, Hkv, Sk, Dh)),
            "wq": _w((lambda *s: torch.randn(*s, dtype=torch.bfloat16) * 1.6)(D, Hq * Dh) * (D ** -0.5)),
            "wo": _w((lambda *s: torch.randn(*s, dtype=torch.bfloat16) * 0.2)(Hq * Dh, D) * ((Hq * Dh) ** -0.5)),
        }
    _A_in = _A_setup()

    def _B_setup():
        B, S, D, F = 1, 1, 512, 1376
        return {
            "x": _w((lambda *s: torch.randn(*s, dtype=torch.bfloat16) * 0.05)(B, S, D)),
            "w_gate": _w((lambda *s: torch.randn(*s, dtype=torch.bfloat16) * 0.1)(D, F)),
            "w_up": _w((lambda *s: torch.randn(*s, dtype=torch.bfloat16) * 0.1)(D, F)),
            "w_down": _w((lambda *s: torch.randn(*s, dtype=torch.bfloat16) * 0.1)(F, D)),
        }
    _B_in = _B_setup()

    return {
        "A_x": _A_in["x"],
        "A_k_cache": _A_in["k_cache"],
        "A_v_cache": _A_in["v_cache"],
        "A_wq": _A_in["wq"],
        "A_wo": _A_in["wo"],
        "B_x": _B_in["x"],
        "B_w_gate": _B_in["w_gate"],
        "B_w_up": _B_in["w_up"],
        "B_w_down": _B_in["w_down"],
    }

def reference(A_x, A_k_cache, A_v_cache, A_wq, A_wo, B_x, B_w_gate, B_w_up, B_w_down):
    def _A_ref(x, k_cache, v_cache, wq, wo):
        B, Sq, D = x.shape
        Hq, Dh = 32, 128
        Hkv, Sk = k_cache.shape[1], k_cache.shape[2]
        W = min(4096, max(256, Sk // 4))
        rep = Hq // Hkv
        q = (lambda x, *d: x.permute(*d))((x @ wq).reshape(B, Sq, Hq, Dh), 0, 2, 1, 3)
        k = (lambda x, n, axis: torch.repeat_interleave(x, n, dim=axis))(k_cache[:, :, Sk - W:, :], rep, axis=1)
        v = (lambda x, n, axis: torch.repeat_interleave(x, n, dim=axis))(v_cache[:, :, Sk - W:, :], rep, axis=1)
        scores = torch.einsum("bhqd,bhkd->bhqk", q, k) * (Dh ** -0.5)
        p = _softmax(scores, axis=-1)
        o = torch.einsum("bhqk,bhkd->bhqd", p, v)
        return (lambda x, *d: x.permute(*d))(o, 0, 2, 1, 3).reshape(B, Sq, Hq * Dh) @ wo

    def _B_ref(x, w_gate, w_up, w_down):
        return (_gelu(x @ w_gate) * (x @ w_up)) @ w_down

    x = A_x
    xn = _layernorm(x, axis=-1)
    s = _A_ref(xn, A_k_cache, A_v_cache, A_wq, A_wo)
    g = _silu(_B_ref(xn, B_w_gate, B_w_up, B_w_down))
    return x + s * g
```

```cpp
#include <hip/hip_runtime.h>
#include <hip/hip_bf16.h>
#include <stdint.h>

typedef unsigned short u16;
__device__ __forceinline__ float bf2f(u16 v) { return __uint_as_float(((unsigned)v) << 16); }
__device__ __forceinline__ float rbf(float f) { return __bfloat162float(__float2bfloat16(f)); }

constexpr int D = 512, H = 32, DH = 128, SK = 32768, W = 4096, F = 1376;

__device__ void layernorm(const u16* x, float* xn, float* red) {
  int t = threadIdx.x;
  float a = bf2f(x[t]), b = bf2f(x[t + 256]);
  red[t] = a + b; __syncthreads();
  for (int s = 128; s > 0; s >>= 1) { if (t < s) red[t] += red[t + s]; __syncthreads(); }
  float mean = red[0] / 512.f; __syncthreads();
  red[t] = (a - mean) * (a - mean) + (b - mean) * (b - mean); __syncthreads();
  for (int s = 128; s > 0; s >>= 1) { if (t < s) red[t] += red[t + s]; __syncthreads(); }
  float var = red[0] / 512.f; __syncthreads();
  float m = rbf(mean), v = rbf(var);
  float den = rbf(sqrtf(rbf(v + 1e-5f)));
  xn[t] = rbf(rbf(a - m) / den);
  xn[t + 256] = rbf(rbf(b - m) / den);
  __syncthreads();
}

__global__ void k1(const u16* x, const u16* wq, const u16* wg, const u16* wu, float* q, float* h) {
  __shared__ float xn[512]; __shared__ float red[256];
  layernorm(x, xn, red);
  int col = blockIdx.x * 256 + threadIdx.x;
  if (col < 4096) {
    float acc = 0;
    for (int k = 0; k < D; ++k) acc += xn[k] * bf2f(wq[(size_t)k * 4096 + col]);
    q[col] = rbf(acc);
  } else if (col - 4096 < F) {
    int c = col - 4096;
    float g = 0, u = 0;
    for (int k = 0; k < D; ++k) { g += xn[k] * bf2f(wg[(size_t)k * F + c]); u += xn[k] * bf2f(wu[(size_t)k * F + c]); }
    g = rbf(g); u = rbf(u);
    float ge = rbf(0.5f * g * (1.f + tanhf(0.7978845608028654f * (g + 0.044715f * g * g * g))));
    h[c] = rbf(ge * u);
  }
}

__global__ void k2(const u16* kc, const u16* vc, const float* q, float* o) {
  __shared__ float sc[W]; __shared__ float red[256]; __shared__ float qs[DH];
  int hh = blockIdx.x, t = threadIdx.x;
  if (t < DH) qs[t] = q[hh * DH + t];
  __syncthreads();
  const u16* K = kc + ((size_t)hh * SK + (SK - W)) * DH;
  const u16* V = vc + ((size_t)hh * SK + (SK - W)) * DH;
  float mx = -INFINITY;
  for (int k = t; k < W; k += 256) {
    float acc = 0;
    for (int d = 0; d < DH; ++d) acc += qs[d] * bf2f(K[(size_t)k * DH + d]);
    acc = rbf(rbf(acc) * 0.08838834764831845f);
    sc[k] = acc; mx = fmaxf(mx, acc);
  }
  red[t] = mx; __syncthreads();
  for (int s = 128; s > 0; s >>= 1) { if (t < s) red[t] = fmaxf(red[t], red[t + s]); __syncthreads(); }
  mx = red[0]; __syncthreads();
  float sum = 0;
  for (int k = t; k < W; k += 256) { float p = expf(sc[k] - mx); sc[k] = p; sum += p; }
  red[t] = sum; __syncthreads();
  for (int s = 128; s > 0; s >>= 1) { if (t < s) red[t] += red[t + s]; __syncthreads(); }
  sum = red[0]; __syncthreads();
  for (int k = t; k < W; k += 256) sc[k] = rbf(sc[k] / sum);
  __syncthreads();
  int d = t & 127, half = t >> 7;
  float acc = 0;
  for (int k = half; k < W; k += 2) acc += sc[k] * bf2f(V[(size_t)k * DH + d]);
  red[t] = acc; __syncthreads();
  if (t < 128) o[hh * DH + t] = rbf(red[t] + red[t + 128]);
}

__global__ void k3(const u16* x, const u16* wo, const u16* wd, const float* o, const float* h, __hip_bfloat16* out) {
  int col = blockIdx.x * 64 + threadIdx.x;
  float s = 0, y = 0;
  for (int k = 0; k < 4096; ++k) s += o[k] * bf2f(wo[(size_t)k * D + col]);
  for (int k = 0; k < F; ++k) y += h[k] * bf2f(wd[(size_t)k * D + col]);
  s = rbf(s); y = rbf(y);
  float g = rbf(y / (1.f + expf(-y)));
  out[col] = __float2bfloat16(bf2f(x[col]) + rbf(s * g));
}

extern "C" void kernel_launch(void* const* d_in, const int* in_sizes, int n_in,
                              void* d_out, int out_size, void* d_ws, size_t ws_size,
                              hipStream_t stream) {
  const u16* x = (const u16*)d_in[0];
  const u16* kc = (const u16*)d_in[1];
  const u16* vc = (const u16*)d_in[2];
  const u16* wq = (const u16*)d_in[3];
  const u16* wo = (const u16*)d_in[4];
  const u16* wg = (const u16*)d_in[6];
  const u16* wu = (const u16*)d_in[7];
  const u16* wd = (const u16*)d_in[8];
  float* q = (float*)d_ws; float* h = q + 4096; float* o = h + 2048;
  k1<<<(4096 + F + 255) / 256, 256, 0, stream>>>(x, wq, wg, wu, q, h);
  k2<<<H, 256, 0, stream>>>(kc, vc, q, o);
  k3<<<8, 64, 0, stream>>>(x, wo, wd, o, h, (__hip_bfloat16*)d_out);
}
```

```cpp
#include <hip/hip_runtime.h>
#include <hip/hip_bf16.h>
#include <stdint.h>

typedef unsigned short u16;
typedef unsigned u32;
typedef unsigned long long u64;
typedef u32 u32x2 __attribute__((ext_vector_type(2)));
typedef u32 u32x4 __attribute__((ext_vector_type(4)));
typedef __bf16 bf16x2 __attribute__((ext_vector_type(2)));
#define GAS __attribute__((address_space(1)))
#define LAS __attribute__((address_space(3)))
#define RLX_AGENT __ATOMIC_RELAXED, __HIP_MEMORY_SCOPE_AGENT
#define RLX_WG __ATOMIC_RELAXED, __HIP_MEMORY_SCOPE_WORKGROUP
#define CFENCE() asm volatile("" ::: "memory")
#define SB() __builtin_amdgcn_sched_barrier(0)

constexpr int DM = 512, NH = 32, DH = 128, SK = 32768, WIN = 4096, FF = 1376;
constexpr u32 HEADB = (u32)SK * DH * 2;
constexpr u32 WOFF = (u32)(SK - WIN) * DH * 2;
constexpr u32 SENT = 0xFFFFFFFFu;
constexpr u32 SPIN_CAP = 1u << 18;
constexpr float LOG2E = 1.4426950408889634f;
constexpr u32 WS_EPOCH = 0, WS_FLAG = 4096, WS_P0 = WS_FLAG + 256 * 128, WS_O1 = WS_P0 + 256 * 512, WS_O2 = WS_O1 + 256 * 1024, WS_END = WS_O2 + 256 * 512;
constexpr u32 WS_STAMP = 1u << 20;

__device__ __forceinline__ float asf(u32 v) { return __builtin_bit_cast(float, v); }
__device__ __forceinline__ u32 asu(float v) { return __builtin_bit_cast(u32, v); }
__device__ __forceinline__ float dot2(u32 a, u32 b, float c) {
  return __builtin_amdgcn_fdot2_f32_bf16(__builtin_bit_cast(bf16x2, a), __builtin_bit_cast(bf16x2, b), c, false);
}
__device__ __forceinline__ u32 pack2(float lo, float hi) { bf16x2 r = {(__bf16)lo, (__bf16)hi}; return __builtin_bit_cast(u32, r); }
__device__ __forceinline__ float rbf(float v) { return (float)(__bf16)v; }
__device__ __forceinline__ float lo16(u32 v) { return asf(v << 16); }
__device__ __forceinline__ float hi16(u32 v) { return asf(v & 0xFFFF0000u); }
template <int CTRL> __device__ __forceinline__ u32 dppu(u32 x) { return (u32)__builtin_amdgcn_mov_dpp((int)x, CTRL, 0xf, 0xf, true); }
template <int CTRL> __device__ __forceinline__ float dpp(float x) { return asf(dppu<CTRL>(asu(x))); }
constexpr int XOR1 = 0xB1, XOR2 = 0x4E, XOR7 = 0x141, XOR15 = 0x140, XOR8 = 0x128, BCAST = 0x150;
__device__ __forceinline__ float swapsum16(float a, float b) {
  auto s = __builtin_amdgcn_permlane16_swap(asu(a), asu(b), false, false); return asf(s[0]) + asf(s[1]);
}
__device__ __forceinline__ float swapsum32(float a, float b) {
  auto s = __builtin_amdgcn_permlane32_swap(asu(a), asu(b), false, false); return asf(s[0]) + asf(s[1]);
}
__device__ __forceinline__ float swapmax16(float a) { auto s = __builtin_amdgcn_permlane16_swap(asu(a), asu(a), false, false); return fmaxf(asf(s[0]), asf(s[1])); }
__device__ __forceinline__ float swapmax32(float a) { auto s = __builtin_amdgcn_permlane32_swap(asu(a), asu(a), false, false); return fmaxf(asf(s[0]), asf(s[1])); }
__device__ __forceinline__ float rowsum(float v) { v += dpp<XOR1>(v); v += dpp<XOR2>(v); v += dpp<XOR7>(v); v += dpp<XOR15>(v); return v; }
__device__ __forceinline__ float rowmax(float v) { v = fmaxf(v, dpp<XOR1>(v)); v = fmaxf(v, dpp<XOR2>(v)); v = fmaxf(v, dpp<XOR7>(v)); v = fmaxf(v, dpp<XOR15>(v)); return v; }
__device__ __forceinline__ float wavesum(float v) { v = rowsum(v); v = swapsum16(v, v); return swapsum32(v, v); }
__device__ __forceinline__ float wavemax(float v) { v = rowmax(v); v = swapmax16(v); return swapmax32(v); }
__device__ __forceinline__ float sum8(float v) { v += dpp<XOR1>(v); v += dpp<XOR2>(v); v += dpp<XOR7>(v); return v; }
__device__ __forceinline__ float max8(float v) { v = fmaxf(v, dpp<XOR1>(v)); v = fmaxf(v, dpp<XOR2>(v)); v = fmaxf(v, dpp<XOR7>(v)); return v; }
__device__ __forceinline__ u32 clampsent(float v) { u32 u = asu(v); return u < 0xFFFFFFFEu ? u : 0xFFFFFFFEu; }
__device__ __forceinline__ __amdgpu_buffer_rsrc_t mkrsrc(const void* p, u32 bytes) { return __builtin_amdgcn_make_buffer_rsrc((void*)p, 0, bytes, 0x00020000); }
__device__ __forceinline__ u32x4 ld16nt(__amdgpu_buffer_rsrc_t r, u32 voff, u32 soff) { return __builtin_amdgcn_raw_buffer_load_b128(r, voff, soff, 2); }
__device__ __forceinline__ u32x2 ld8nt(__amdgpu_buffer_rsrc_t r, u32 voff, u32 soff) { return __builtin_amdgcn_raw_buffer_load_b64(r, voff, soff, 2); }
__device__ __forceinline__ u64 ldg64(const char* p) { return __hip_atomic_load((GAS const u64*)p, RLX_AGENT); }
__device__ __forceinline__ void stg64(char* p, u64 v) { __hip_atomic_store((GAS u64*)p, v, RLX_AGENT); }
__device__ __forceinline__ void stg32(char* p, u32 v) { __hip_atomic_store((GAS u32*)p, v, RLX_AGENT); }
__device__ __forceinline__ u64 mk64(u32 lo, u32 hi) { return (u64)lo | ((u64)hi << 32); }
#define STAMPAT(k) do {} while (0)
#define STAMPDECL do {} while (0)
#define STAMPFLUSH() do {} while (0)

template <int N> __device__ __forceinline__ float gemv_tail(u32 pairs, const u32 (&Wp)[N][4]) {
  float a0 = 0.f, a1 = 0.f, a2 = 0.f, a3 = 0.f;
#define GSTEP(I) if constexpr ((I) < N) { const u32 pb = dppu<BCAST + (I)>(pairs); a0 = dot2(pb, Wp[I][0], a0); a1 = dot2(pb, Wp[I][1], a1); a2 = dot2(pb, Wp[I][2], a2); a3 = dot2(pb, Wp[I][3], a3); }
  GSTEP(0) GSTEP(1) GSTEP(2) GSTEP(3) GSTEP(4) GSTEP(5) GSTEP(6) GSTEP(7) GSTEP(8) GSTEP(9) GSTEP(10) GSTEP(11) GSTEP(12) GSTEP(13) GSTEP(14) GSTEP(15)
#undef GSTEP
  const float b0 = swapsum32(a0, a2), b1 = swapsum32(a1, a3);
  return swapsum16(b0, b1);
}
template <int N> __device__ __forceinline__ void load_pairs(__amdgpu_buffer_rsrc_t rs, u32 voff, u32x2 (&A)[N], u32x2 (&B)[N]) {
#pragma unroll
  for (int i = 0; i < N; ++i) { A[i] = ld8nt(rs, voff + i * 8192u, 0); B[i] = ld8nt(rs, voff + i * 8192u + 1024u, 0); }
}
template <int N> __device__ __forceinline__ void pack_pairs(const u32x2 (&A)[N], const u32x2 (&B)[N], u32 (&Wp)[N][4]) {
#pragma unroll
  for (int i = 0; i < N; ++i) {
    Wp[i][0] = __builtin_amdgcn_perm(B[i].x, A[i].x, 0x05040100u); Wp[i][1] = __builtin_amdgcn_perm(B[i].x, A[i].x, 0x07060302u);
    Wp[i][2] = __builtin_amdgcn_perm(B[i].y, A[i].y, 0x05040100u); Wp[i][3] = __builtin_amdgcn_perm(B[i].y, A[i].y, 0x07060302u);
  }
}
__device__ __forceinline__ float gemv_front(const u32x4 (&Wt)[16], const u32 (&xv)[16], int lane) {
  float a[8];
#pragma unroll
  for (int j = 0; j < 8; ++j) a[j] = 0.f;
#pragma unroll
  for (int i = 0; i < 16; ++i) {
    const u32 xl = xv[i], xh = xv[i] << 16;
#pragma unroll
    for (int j = 0; j < 4; ++j) { a[2 * j] = dot2(Wt[i][j], xl, a[2 * j]); a[2 * j + 1] = dot2(Wt[i][j], xh, a[2 * j + 1]); }
  }
  float b[4];
#pragma unroll
  for (int j = 0; j < 4; ++j) { const float t0 = a[j] + dpp<XOR8>(a[j]), t1 = a[j + 4] + dpp<XOR8>(a[j + 4]); b[j] = (lane & 8) ? t1 : t0; }
  const float c0 = swapsum16(b[0], b[2]), c1 = swapsum16(b[1], b[3]);
  return swapsum32(c0, c1);
}

__global__ void __launch_bounds__(576, 3) fused_decode(const u16* __restrict__ x, const u16* __restrict__ kc, const u16* __restrict__ vc,
                                                       const u16* __restrict__ wq, const u16* __restrict__ wo, const u16* __restrict__ wg,
                                                       const u16* __restrict__ wu, const u16* __restrict__ wd, u16* __restrict__ out, char* __restrict__ ws) {
  __shared__ __attribute__((aligned(16))) u32 lds[64 + 4 + 256 + 64 + 8 * 128 + 16];
  u32* const qs = lds;
  u32* const qflag = lds + 64;
  u32* const xs = lds + 68;
  u32* const hs = xs + 256;
  u32* const part = hs + 64;
  u32* const ml = part + 8 * 128;
  const int b = blockIdx.x, idx = b >> 3;
  const int h = (b & 7) * 4 + (idx >> 3), s = idx & 7, g = h * 8 + s;
  const int lane = threadIdx.x & 63;
  const int wave = __builtin_amdgcn_readfirstlane(threadIdx.x >> 6);
  const int r = lane >> 4, c = lane & 15;
  STAMPDECL;
  STAMPAT(0);

  if (wave != 0) {
    const int w = wave - 1;
    const size_t base = (size_t)h * HEADB + WOFF + (u32)s * 131072u + (u32)w * 16384u;
    const __amdgpu_buffer_rsrc_t rk = mkrsrc((const char*)kc + base, 16384), rv = mkrsrc((const char*)vc + base, 16384);
    const u32 voff = lane * 16, rot = idx + 4 * w;
    u32x4 K[16], V[16];
#pragma unroll
    for (int i = 0; i < 2; ++i) K[i] = ld16nt(rk, voff, ((i + rot) & 15) * 1024u);
    SB();
    __builtin_amdgcn_s_barrier();
    SB();
#pragma unroll
    for (int i = 2; i < 16; ++i) K[i] = ld16nt(rk, voff, ((i + rot) & 15) * 1024u);
#pragma unroll
    for (int i = 0; i < 16; ++i) V[i] = ld16nt(rv, voff, ((i + rot) & 15) * 1024u);
    SB();
    STAMPAT(1);
    for (u32 spin = 0; __hip_atomic_load((LAS u32*)qflag, RLX_WG) == 0u && spin < SPIN_CAP * 16; ++spin) __builtin_amdgcn_s_sleep(2);
    CFENCE();
    const u32x4 q4 = *(const u32x4*)(qs + 4 * c);
    STAMPAT(2);
    float xsc[16];
#pragma unroll
    for (int i = 0; i < 16; ++i) xsc[i] = dot2(K[i][3], q4[3], dot2(K[i][2], q4[2], dot2(K[i][1], q4[1], dot2(K[i][0], q4[0], 0.f))));
#define TR_STEP(HALF, CTRL, BIT) _Pragma("unroll") for (int k = 0; k < (HALF); ++k) { const float t0 = xsc[k] + dpp<CTRL>(xsc[k]), t1 = xsc[k + (HALF)] + dpp<CTRL>(xsc[k + (HALF)]); xsc[k] = (c & (BIT)) ? t1 : t0; }
    TR_STEP(8, XOR8, 8) TR_STEP(4, XOR7, 4) TR_STEP(2, XOR2, 2) TR_STEP(1, XOR1, 1)
#undef TR_STEP
    const float sc = rbf(rbf(xsc[0]) * 0.08838834764831845f);
    const float m = wavemax(sc);
    const float p = __builtin_amdgcn_exp2f((sc - m) * LOG2E);
    const u32 pl = pack2(p, 0.f), ph = pl << 16;
    const float l = wavesum(p);
    STAMPAT(3);
    float a[8];
#pragma unroll
    for (int j = 0; j < 8; ++j) a[j] = 0.f;
#define PV(I) { const u32 bl = dppu<BCAST + (I)>(pl), bh = dppu<BCAST + (I)>(ph); _Pragma("unroll") for (int j = 0; j < 4; ++j) { a[2 * j] = dot2(V[I][j], bl, a[2 * j]); a[2 * j + 1] = dot2(V[I][j], bh, a[2 * j + 1]); } }
    PV(0) PV(1) PV(2) PV(3) PV(4) PV(5) PV(6) PV(7) PV(8) PV(9) PV(10) PV(11) PV(12) PV(13) PV(14) PV(15)
#undef PV
    const float b0 = swapsum32(a[0], a[4]), b1 = swapsum32(a[1], a[5]), b2 = swapsum32(a[2], a[6]), b3 = swapsum32(a[3], a[7]);
    const float o0 = swapsum16(b0, b2), o1 = swapsum16(b1, b3);
    *(u32x2*)(part + w * 128 + lane * 2) = u32x2{asu(o0), asu(o1)};
    if (lane == 0) *(u32x2*)(ml + w * 2) = u32x2{asu(m), asu(l)};
    STAMPAT(4);
    __syncthreads();
    STAMPFLUSH();
    return;
  }

  __builtin_amdgcn_s_setprio(3);
  const int rq = s >> 1, ch = s & 1, r8 = lane >> 3, c8 = lane & 7;
  const u32 cu = (u32)(6 * h) + (u32)c8;
  const u32 vf0 = ((c8 < 6) & (cu < FF / 8)) ? (u32)r8 * (FF * 2) + cu * 16u : 0x80000000u;
  const u64 epoch = ldg64(ws + WS_EPOCH);
  const u32x4 x4 = ld16nt(mkrsrc(x, 1024), lane * 16, 0);
  const u32 xo = __builtin_amdgcn_raw_buffer_load_b32(mkrsrc(x, 1024), (64 * s + 2 * h) * 2, 0, 2);
  u32x4 Wa[16], Wb[16];
  {
    const __amdgpu_buffer_rsrc_t rq_ = mkrsrc((const char*)wq + (size_t)(128 * rq) * 8192 + h * 256 + ch * 128, 127 * 8192 + 128);
    const u32 vo = r8 * 8192 + c8 * 16;
#pragma unroll
    for (int i = 0; i < 16; ++i) Wa[i] = ld16nt(rq_, vo, i * 65536u);
    const __amdgpu_buffer_rsrc_t rf_ = mkrsrc((const char*)(ch ? wu : wg) + (size_t)(128 * rq) * (FF * 2), 128 * FF * 2);
    const u32 vf = vf0;
#pragma unroll
    for (int i = 0; i < 16; ++i) Wb[i] = ld16nt(rf_, vf, i * (8u * FF * 2));
  }
  if (lane == 0) *qflag = 0u;
  stg64(ws + WS_O1 + g * 1024 + lane * 8, mk64(SENT, SENT));
  stg64(ws + WS_O2 + g * 512 + lane * 8, mk64(SENT, SENT));
  if (lane == 0) stg64(ws + WS_O1 + g * 1024 + 512, mk64(SENT, SENT));
  SB();
  asm volatile("s_waitcnt lgkmcnt(0)" ::: "memory");
  __builtin_amdgcn_s_barrier();
  SB();
  u32 xv[16];
  {
    float xf[8];
#pragma unroll
    for (int j = 0; j < 4; ++j) { xf[2 * j] = lo16(x4[j]); xf[2 * j + 1] = hi16(x4[j]); }
    float sum = 0.f;
#pragma unroll
    for (int j = 0; j < 8; ++j) sum += xf[j];
    const float mean = wavesum(sum) * (1.f / 512.f);
    float sq = 0.f;
#pragma unroll
    for (int j = 0; j < 8; ++j) sq = fmaf(xf[j] - mean, xf[j] - mean, sq);
    const float var = wavesum(sq) * (1.f / 512.f);
    const float mb = rbf(mean), den = rbf(sqrtf(rbf(rbf(var) + 1e-5f)));
    u32x4 xn;
#pragma unroll
    for (int j = 0; j < 4; ++j) xn[j] = pack2(rbf(xf[2 * j] - mb) / den, rbf(xf[2 * j + 1] - mb) / den);
    *(u32x4*)(xs + lane * 4) = xn;
    CFENCE();
#pragma unroll
    for (int i = 0; i < 16; ++i) xv[i] = ((const u16*)xs)[(16 * rq + i) * 8 + r8];
  }
  STAMPAT(1);
  {
    const float pq = gemv_front(Wa, xv, lane), pf = gemv_front(Wb, xv, lane);
    const int col = 8 * c8 + (lane >> 5) + 2 * ((lane >> 4) & 1) + 4 * ((lane >> 3) & 1);
    asm volatile("" :: "v"(pq), "v"(pf));
    STAMPAT(11);
    stg32(ws + WS_P0 + g * 512 + col * 4, asu(pq));
    stg32(ws + WS_P0 + g * 512 + 256 + col * 4, asu(pf));
  }
  asm volatile("s_waitcnt vmcnt(0)" ::: "memory");
  const u64 tag = epoch + 1;
  if (lane == 0) stg64(ws + WS_FLAG + g * 128, tag);
  STAMPAT(2);
  for (u32 spin = 0; spin < SPIN_CAP; ++spin) {
    CFENCE();
    if (__all(ldg64(ws + WS_FLAG + (h * 8 + (lane & 7)) * 128) == tag)) break;
    __builtin_amdgcn_s_sleep(1);
  }
  CFENCE();
  STAMPAT(3);
  u32 hp;
  {
    const __amdgpu_buffer_rsrc_t rp = mkrsrc(ws + WS_P0 + h * 8 * 512, 8 * 512);
    const u32 vo = r8 >= 4 ? 512 + (lane & 31) * 16 : (lane & 31) * 16;
    u32x4 t[4];
#pragma unroll
    for (int k = 0; k < 4; ++k) t[k] = __builtin_amdgcn_raw_buffer_load_b128(rp, vo, k * 1024, 16);
    float v[4];
#pragma unroll
    for (int j = 0; j < 4; ++j) v[j] = ((asf(t[0][j]) + asf(t[1][j])) + asf(t[2][j])) + asf(t[3][j]);
    if ((lane & 16) == 0) *(u32x2*)(qs + (lane >> 5) * 32 + (lane & 15) * 2) = u32x2{pack2(v[0], v[1]), pack2(v[2], v[3])};
    CFENCE();
    if (lane == 0) __hip_atomic_store((LAS u32*)qflag, 1u, RLX_WG);
    STAMPAT(4);
    float hv[4];
#pragma unroll
    for (int j = 0; j < 4; ++j) {
      auto sw = __builtin_amdgcn_permlane32_swap(asu(v[j]), asu(v[j]), false, false);
      const float gt = rbf(asf(sw[0])), up = rbf(asf(sw[1]));
      const float u = 0.7978845608028654f * (gt + 0.044715f * gt * gt * gt);
      const float th = 1.f - 2.f / (__expf(2.f * u) + 1.f);
      hv[j] = rbf(0.5f * gt * (1.f + th)) * up;
    }
    if (lane >= 16 && lane < 32) *(u32x2*)(hs + (lane - 16) * 2) = u32x2{pack2(hv[0], hv[1]), pack2(hv[2], hv[3])};
    CFENCE();
    hp = hs[(4 * c + r) & 31];
  }
  u32 Wo[16][4];
  {
    u32x2 A[6], B[6];
    load_pairs<6>(mkrsrc(wd, FF * 1024), (48 * h + 2 * r) * 1024 + s * 128 + c * 8, A, B);
    u32x2 C[16], Dd[16];
    load_pairs<16>(mkrsrc(wo, 4096 * 1024), (128 * h + 2 * r) * 1024 + s * 128 + c * 8, C, Dd);
    u32 Wd[6][4];
    pack_pairs<6>(A, B, Wd);
    const float y = gemv_tail<6>(hp, Wd);
    stg32(ws + WS_O2 + g * 512 + 256 + (4 * c + r) * 4, clampsent(y));
    pack_pairs<16>(C, Dd, Wo);
  }
  for (u32 spin = 0; spin < SPIN_CAP; ++spin) {
    CFENCE();
    if (__all(ldg64(ws + WS_FLAG + ((lane & 31) * 8 + s) * 128) == tag)) break;
    __builtin_amdgcn_s_sleep(1);
  }
  STAMPAT(5);
  __syncthreads();
  STAMPAT(6);
  {
    const u32x2 t = *(const u32x2*)(ml + (lane & 7) * 2);
    u32x2 po[8];
#pragma unroll
    for (int k = 0; k < 8; ++k) po[k] = *(const u32x2*)(part + k * 128 + lane * 2);
    const float M = max8(asf(t.x));
    const float f = __builtin_amdgcn_exp2f((asf(t.x) - M) * LOG2E);
    const float L = sum8(f * asf(t.y));
    float o0 = 0.f, o1 = 0.f;
#define MRG(I) { const float fb = dpp<BCAST + (I)>(f); o0 = fmaf(fb, asf(po[I].x), o0); o1 = fmaf(fb, asf(po[I].y), o1); }
    MRG(0) MRG(1) MRG(2) MRG(3) MRG(4) MRG(5) MRG(6) MRG(7)
    stg64(ws + WS_O1 + g * 1024 + lane * 8, mk64(clampsent(o0), clampsent(o1)));
    if (lane == 0) stg64(ws + WS_O1 + g * 1024 + 512, mk64(clampsent(M), clampsent(L)));
  }
  STAMPAT(7);
  u32 op;
  {
    u64 t, po[8];
    const char* p1 = ws + WS_O1 + h * 8192;
    for (u32 spin = 0; spin < SPIN_CAP; ++spin) {
      CFENCE();
      t = ldg64(p1 + (lane & 7) * 1024 + 512);
#pragma unroll
      for (int k = 0; k < 8; ++k) po[k] = ldg64(p1 + k * 1024 + lane * 8);
      u32 mx = max((u32)t, (u32)(t >> 32));
#pragma unroll
      for (int k = 0; k < 8; ++k) mx = max(mx, max((u32)po[k], (u32)(po[k] >> 32)));
      if (__all(mx != SENT)) break;
    }
    const float mk = asf((u32)t), lk = asf((u32)(t >> 32));
    const float M = max8(mk);
    const float f = __builtin_amdgcn_exp2f((mk - M) * LOG2E);
    const float L = sum8(f * lk);
    float o0 = 0.f, o1 = 0.f;
#undef MRG
#define MRG(I) { const float fb = dpp<BCAST + (I)>(f); o0 = fmaf(fb, asf((u32)po[I]), o0); o1 = fmaf(fb, asf((u32)(po[I] >> 32)), o1); }
    MRG(0) MRG(1) MRG(2) MRG(3) MRG(4) MRG(5) MRG(6) MRG(7)
#undef MRG
    const float inv = 1.f / L;
    op = pack2(o0 * inv, o1 * inv);
  }
  STAMPAT(8);
  stg32(ws + WS_O2 + g * 512 + (4 * c + r) * 4, clampsent(gemv_tail<16>(op, Wo)));
  STAMPAT(9);
  {
    u64 t;
    const char* p2 = ws + WS_O2 + ((lane & 31) * 8 + s) * 512 + (lane >> 5) * 256 + h * 8;
    for (u32 spin = 0; spin < SPIN_CAP; ++spin) {
      CFENCE();
      t = ldg64(p2);
      if (__all(max((u32)t, (u32)(t >> 32)) != SENT)) break;
    }
    float v0 = rowsum(asf((u32)t)), v1 = rowsum(asf((u32)(t >> 32)));
    v0 = swapsum16(v0, v0); v1 = swapsum16(v1, v1);
    auto s0 = __builtin_amdgcn_permlane32_swap(asu(v0), asu(v0), false, false);
    auto s1 = __builtin_amdgcn_permlane32_swap(asu(v1), asu(v1), false, false);
    const float sa = rbf(asf(s0[0])), sb = rbf(asf(s1[0])), ya = rbf(asf(s0[1])), yb = rbf(asf(s1[1]));
    const float ga = rbf(ya / (1.f + __expf(-ya))), gb = rbf(yb / (1.f + __expf(-yb)));
    const u32 res = pack2(lo16(xo) + rbf(sa * ga), hi16(xo) + rbf(sb * gb));
    if (lane == 0) {
      *(u32*)(out + 64 * s + 2 * h) = res;
      if (g == 0) stg64(ws + WS_EPOCH, tag);
    }
  }
  STAMPAT(10);
  STAMPFLUSH();
}

extern "C" void kernel_launch(void* const* d_in, const int* in_sizes, int n_in, void* d_out, int out_size, void* d_ws, size_t ws_size, hipStream_t stream) {
  fused_decode<<<256, 576, 0, stream>>>((const u16*)d_in[0], (const u16*)d_in[1], (const u16*)d_in[2], (const u16*)d_in[3], (const u16*)d_in[4],
                                        (const u16*)d_in[6], (const u16*)d_in[7], (const u16*)d_in[8], (u16*)d_out, (char*)d_ws);
}
```

```cpp
#include <hip/hip_runtime.h>
#include <hip/hip_bf16.h>
#include <stdint.h>

typedef unsigned short u16;
typedef unsigned u32;
typedef unsigned long long u64;
typedef u32 u32x2 __attribute__((ext_vector_type(2)));
typedef u32 u32x4 __attribute__((ext_vector_type(4)));
typedef __bf16 bf16x2 __attribute__((ext_vector_type(2)));
#define GAS __attribute__((address_space(1)))
#define LAS __attribute__((address_space(3)))
#define RLX_AGENT __ATOMIC_RELAXED, __HIP_MEMORY_SCOPE_AGENT
#define RLX_WG __ATOMIC_RELAXED, __HIP_MEMORY_SCOPE_WORKGROUP
#define CFENCE() asm volatile("" ::: "memory")
#define SB() __builtin_amdgcn_sched_barrier(0)

constexpr int DM = 512, NH = 32, DH = 128, SK = 32768, WIN = 4096, FF = 1376;
constexpr u32 HEADB = (u32)SK * DH * 2;
constexpr u32 WOFF = (u32)(SK - WIN) * DH * 2;
constexpr u32 SPIN_CAP = 1u << 18;
constexpr float LOG2E = 1.4426950408889634f;
constexpr u32 WS_EPOCH = 0, WS_O1 = 4096, WS_ML = WS_O1 + 256 * 1024, WS_PF = WS_ML + 256 * 16, WS_O2 = WS_PF + 256 * 512, WS_PQ = WS_O2 + 256 * 1024, WS_END = WS_PQ + 256 * 512;
constexpr u32 WS_STAMP = 2u << 20;
#define KPRE 2
#define KWIN 3
#define KTHR 16
#define XAUX 0
#define PSL0 8
#define PSL00 40
#define H2PROD 0
#define DEFER 1
#define PSL1 1
#define PSL2 1

__device__ __forceinline__ float asf(u32 v) { return __builtin_bit_cast(float, v); }
__device__ __forceinline__ u32 asu(float v) { return __builtin_bit_cast(u32, v); }
__device__ __forceinline__ float dot2(u32 a, u32 b, float c) {
  return __builtin_amdgcn_fdot2_f32_bf16(__builtin_bit_cast(bf16x2, a), __builtin_bit_cast(bf16x2, b), c, false);
}
__device__ __forceinline__ u32 pack2(float lo, float hi) { bf16x2 r = {(__bf16)lo, (__bf16)hi}; return __builtin_bit_cast(u32, r); }
__device__ __forceinline__ float rbf(float v) { return (float)(__bf16)v; }
__device__ __forceinline__ float lo16(u32 v) { return asf(v << 16); }
__device__ __forceinline__ float hi16(u32 v) { return asf(v & 0xFFFF0000u); }
template <int CTRL> __device__ __forceinline__ u32 dppu(u32 x) { return (u32)__builtin_amdgcn_mov_dpp((int)x, CTRL, 0xf, 0xf, true); }
template <int CTRL> __device__ __forceinline__ float dpp(float x) { return asf(dppu<CTRL>(asu(x))); }
constexpr int XOR1 = 0xB1, XOR2 = 0x4E, XOR7 = 0x141, XOR15 = 0x140, XOR8 = 0x128, BCAST = 0x150;
__device__ __forceinline__ float swapsum16(float a, float b) {
  auto s = __builtin_amdgcn_permlane16_swap(asu(a), asu(b), false, false); return asf(s[0]) + asf(s[1]);
}
__device__ __forceinline__ float swapsum32(float a, float b) {
  auto s = __builtin_amdgcn_permlane32_swap(asu(a), asu(b), false, false); return asf(s[0]) + asf(s[1]);
}
__device__ __forceinline__ float swapmax16(float a) { auto s = __builtin_amdgcn_permlane16_swap(asu(a), asu(a), false, false); return fmaxf(asf(s[0]), asf(s[1])); }
__device__ __forceinline__ float swapmax32(float a) { auto s = __builtin_amdgcn_permlane32_swap(asu(a), asu(a), false, false); return fmaxf(asf(s[0]), asf(s[1])); }
__device__ __forceinline__ float rowsum(float v) { v += dpp<XOR1>(v); v += dpp<XOR2>(v); v += dpp<XOR7>(v); v += dpp<XOR15>(v); return v; }
__device__ __forceinline__ float rowmax(float v) { v = fmaxf(v, dpp<XOR1>(v)); v = fmaxf(v, dpp<XOR2>(v)); v = fmaxf(v, dpp<XOR7>(v)); v = fmaxf(v, dpp<XOR15>(v)); return v; }
__device__ __forceinline__ float wavesum(float v) { v = rowsum(v); v = swapsum16(v, v); return swapsum32(v, v); }
__device__ __forceinline__ float wavemax(float v) { v = rowmax(v); v = swapmax16(v); return swapmax32(v); }
__device__ __forceinline__ float sum8(float v) { v += dpp<XOR1>(v); v += dpp<XOR2>(v); v += dpp<XOR7>(v); return v; }
__device__ __forceinline__ float max8(float v) { v = fmaxf(v, dpp<XOR1>(v)); v = fmaxf(v, dpp<XOR2>(v)); v = fmaxf(v, dpp<XOR7>(v)); return v; }
__device__ __forceinline__ __amdgpu_buffer_rsrc_t mkrsrc(const void* p, u32 bytes) { return __builtin_amdgcn_make_buffer_rsrc((void*)p, 0, bytes, 0x00020000); }
__device__ __forceinline__ u32x4 ld16nt(__amdgpu_buffer_rsrc_t r, u32 voff, u32 soff) { return __builtin_amdgcn_raw_buffer_load_b128(r, voff, soff, 2); }
__device__ __forceinline__ u32x2 ld8nt(__amdgpu_buffer_rsrc_t r, u32 voff, u32 soff) { return __builtin_amdgcn_raw_buffer_load_b64(r, voff, soff, 2); }
__device__ __forceinline__ u64 ldg64(const char* p) { return __hip_atomic_load((GAS const u64*)p, RLX_AGENT); }
__device__ __forceinline__ void stg64(char* p, u64 v) { __hip_atomic_store((GAS u64*)p, v, RLX_AGENT); }
__device__ __forceinline__ void publish2(__amdgpu_buffer_rsrc_t r, u32 off, float a, float b, u32 tag) {
  __builtin_amdgcn_raw_buffer_store_b128(u32x4{asu(a), tag, asu(b), tag}, r, off, 0, 16);
}
__device__ __forceinline__ void publish1(__amdgpu_buffer_rsrc_t r, u32 off, float a, u32 tag) {
  __builtin_amdgcn_raw_buffer_store_b64(u32x2{asu(a), tag}, r, off, 0, 16);
}
__device__ __forceinline__ u32x4 gather2(__amdgpu_buffer_rsrc_t r, u32 voff, u32 soff) { return __builtin_amdgcn_raw_buffer_load_b128(r, voff, soff, 16); }
__device__ __forceinline__ u32x2 gather1(__amdgpu_buffer_rsrc_t r, u32 voff, u32 soff) { return __builtin_amdgcn_raw_buffer_load_b64(r, voff, soff, 16); }
__device__ __forceinline__ bool bad2(u32x4 v, u32 tag) { return ((v[1] ^ tag) | (v[3] ^ tag)) != 0u; }
__device__ __forceinline__ void lds_wait(u32* p, u32 want) {
  for (u32 spin = 0; __hip_atomic_load((LAS u32*)p, RLX_WG) < want && spin < SPIN_CAP * 16; ++spin) __builtin_amdgcn_s_sleep(1);
  CFENCE();
}
__device__ __forceinline__ void lds_signal(u32* p, int lane) {
  CFENCE();
  if (lane == 0) __hip_atomic_fetch_add((LAS u32*)p, 1u, RLX_WG);
  CFENCE();
}
#define STAMPAT(k) do {} while (0)
#define STAMPDECL do {} while (0)
#define STAMPFLUSH() do {} while (0)

template <int N> __device__ __forceinline__ float gemv_tail(u32 pairs, const u32 (&Wp)[N][4]) {
  float a0 = 0.f, a1 = 0.f, a2 = 0.f, a3 = 0.f;
#define GSTEP(I) if constexpr ((I) < N) { const u32 pb = dppu<BCAST + (I)>(pairs); a0 = dot2(pb, Wp[I][0], a0); a1 = dot2(pb, Wp[I][1], a1); a2 = dot2(pb, Wp[I][2], a2); a3 = dot2(pb, Wp[I][3], a3); }
  GSTEP(0) GSTEP(1) GSTEP(2) GSTEP(3) GSTEP(4) GSTEP(5) GSTEP(6) GSTEP(7) GSTEP(8) GSTEP(9) GSTEP(10) GSTEP(11) GSTEP(12) GSTEP(13) GSTEP(14) GSTEP(15)
#undef GSTEP
  const float b0 = swapsum32(a0, a2), b1 = swapsum32(a1, a3);
  return swapsum16(b0, b1);
}
template <int N> __device__ __forceinline__ void load_pairs(__amdgpu_buffer_rsrc_t rs, u32 voff, u32x2 (&A)[N], u32x2 (&B)[N]) {
#pragma unroll
  for (int i = 0; i < N; ++i) { A[i] = ld8nt(rs, voff + i * 8192u, 0); B[i] = ld8nt(rs, voff + i * 8192u + 1024u, 0); }
}
template <int N> __device__ __forceinline__ void pack_pairs(const u32x2 (&A)[N], const u32x2 (&B)[N], u32 (&Wp)[N][4]) {
#pragma unroll
  for (int i = 0; i < N; ++i) {
    Wp[i][0] = __builtin_amdgcn_perm(B[i].x, A[i].x, 0x05040100u); Wp[i][1] = __builtin_amdgcn_perm(B[i].x, A[i].x, 0x07060302u);
    Wp[i][2] = __builtin_amdgcn_perm(B[i].y, A[i].y, 0x05040100u); Wp[i][3] = __builtin_amdgcn_perm(B[i].y, A[i].y, 0x07060302u);
    asm volatile("" : "+v"(Wp[i][0]), "+v"(Wp[i][1]), "+v"(Wp[i][2]), "+v"(Wp[i][3]));
  }
}
__device__ __forceinline__ void gemv_rows(const u32x4 (&Wt)[16], const u32 (&xl)[16], float (&a)[8]) {
#pragma unroll
  for (int j = 0; j < 8; ++j) a[j] = 0.f;
#pragma unroll
  for (int i = 0; i < 16; ++i) {
    const u32 xh = xl[i] << 16;
#pragma unroll
    for (int j = 0; j < 4; ++j) { a[2 * j] = dot2(Wt[i][j], xl[i], a[2 * j]); a[2 * j + 1] = dot2(Wt[i][j], xh, a[2 * j + 1]); }
  }
}
__device__ __forceinline__ void reduce_rows(const float (&a)[8], float& o0, float& o1) {
  const float b0 = swapsum32(a[0], a[4]), b1 = swapsum32(a[1], a[5]), b2 = swapsum32(a[2], a[6]), b3 = swapsum32(a[3], a[7]);
  o0 = swapsum16(b0, b2); o1 = swapsum16(b1, b3);
}

__device__ __forceinline__ float reduce_rows8(const float (&a)[8], int lane) {
  float bq[4];
#pragma unroll
  for (int j = 0; j < 4; ++j) { const float t0 = a[j] + dpp<XOR8>(a[j]), t1 = a[j + 4] + dpp<XOR8>(a[j + 4]); bq[j] = (lane & 8) ? t1 : t0; }
  const float c0 = swapsum16(bq[0], bq[2]), c1 = swapsum16(bq[1], bq[3]);
  return swapsum32(c0, c1);
}

constexpr int L_CTL = 0;
constexpr int L_XT = 4;
constexpr int L_QS = L_XT + 256;
constexpr int L_OP = L_QS + 64;
constexpr int L_ML = L_OP + 1024;
constexpr int L_HS = L_ML + 16;
constexpr int L_END = L_HS + 32;

__global__ void __launch_bounds__(640, 3) fused_decode(const u16* __restrict__ x, const u16* __restrict__ kc, const u16* __restrict__ vc,
                                                       const u16* __restrict__ wq, const u16* __restrict__ wo, const u16* __restrict__ wg,
                                                       const u16* __restrict__ wu, const u16* __restrict__ wd, u16* out, char* ws) {
  __shared__ __attribute__((aligned(16))) u32 lds[L_END];
  const int b = blockIdx.x, idx = b >> 3;
  const int h = (b & 7) * 4 + (idx >> 3), s = idx & 7, g = h * 8 + s;
  const int lane = threadIdx.x & 63;
  const int wave = __builtin_amdgcn_readfirstlane(threadIdx.x >> 6);
  const int r = lane >> 4, c = lane & 15;
  STAMPDECL;
  STAMPAT(0);

  if (wave >= 2) {
    const int w = wave - 2;
    const size_t base = (size_t)h * HEADB + WOFF + (u32)s * 131072u + (u32)w * 16384u;
    const __amdgpu_buffer_rsrc_t rk = mkrsrc((const char*)kc + base, 16384), rv = mkrsrc((const char*)vc + base, 16384);
    const u32 voff = lane * 16, rot = idx + 4 * w;
    u32x4 K[16], V[16];
#pragma unroll
    for (int i = 0; i < KPRE; ++i) K[i] = ld16nt(rk, voff, ((i + rot) & 15) * 1024u);
    SB();
    __builtin_amdgcn_s_barrier();
    SB();
#pragma unroll
    for (int i = KPRE; i < 16; ++i) { K[i] = ld16nt(rk, voff, ((i + rot) & 15) * 1024u); if (i < KTHR) { SB(); asm volatile("s_waitcnt vmcnt(%0)" :: "i"(KWIN - 1) : "memory"); SB(); } }
#pragma unroll
    for (int i = 0; i < 16; ++i) V[i] = ld16nt(rv, voff, ((i + rot) & 15) * 1024u);
    SB();
    lds_signal(lds + L_CTL + 2, lane);
    STAMPAT(3);
    lds_wait(lds + L_CTL + 1, 1);
    const u32x4 q4 = *(const u32x4*)(lds + L_QS + 4 * c);
    STAMPAT(4);
    float xsc[16];
#pragma unroll
    for (int i = 0; i < 16; ++i) xsc[i] = dot2(K[i][3], q4[3], dot2(K[i][2], q4[2], dot2(K[i][1], q4[1], dot2(K[i][0], q4[0], 0.f))));
#define TR_STEP(HALF, CTRL, BIT) _Pragma("unroll") for (int k = 0; k < (HALF); ++k) { const float t0 = xsc[k] + dpp<CTRL>(xsc[k]), t1 = xsc[k + (HALF)] + dpp<CTRL>(xsc[k + (HALF)]); xsc[k] = (c & (BIT)) ? t1 : t0; }
    TR_STEP(8, XOR8, 8) TR_STEP(4, XOR7, 4) TR_STEP(2, XOR2, 2) TR_STEP(1, XOR1, 1)
#undef TR_STEP
    const float sc = rbf(rbf(xsc[0]) * 0.08838834764831845f);
    const float m = wavemax(sc);
    const float p = __builtin_amdgcn_exp2f((sc - m) * LOG2E);
    const u32 pl = pack2(p, 0.f), ph = pl << 16;
    const float l = wavesum(p);
    STAMPAT(5);
    float a[8];
#pragma unroll
    for (int j = 0; j < 8; ++j) a[j] = 0.f;
#define PV(I) { const u32 bl = dppu<BCAST + (I)>(pl), bh = dppu<BCAST + (I)>(ph); _Pragma("unroll") for (int j = 0; j < 4; ++j) { a[2 * j] = dot2(V[I][j], bl, a[2 * j]); a[2 * j + 1] = dot2(V[I][j], bh, a[2 * j + 1]); } }
    PV(0) PV(1) PV(2) PV(3) PV(4) PV(5) PV(6) PV(7) PV(8) PV(9) PV(10) PV(11) PV(12) PV(13) PV(14) PV(15)
#undef PV
    float o0, o1;
    reduce_rows(a, o0, o1);
    *(u32x2*)(lds + L_OP + w * 128 + lane * 2) = u32x2{asu(o0), asu(o1)};
    if (lane == 0) *(u32x2*)(lds + L_ML + w * 2) = u32x2{asu(m), asu(l)};
    STAMPAT(6);
    __syncthreads();
    STAMPFLUSH();
    return;
  }

  __builtin_amdgcn_s_setprio(3);
  const __amdgpu_buffer_rsrc_t rws = mkrsrc(ws, WS_END);
  if (wave == 1) {
    const int rq = s >> 1, ch = s & 1, r8 = lane >> 3, c8 = lane & 7;
    const u32 cu = (u32)__builtin_amdgcn_readfirstlane(6 * h) + (u32)c8;
    u32 vf = ((c8 < 6) & (cu < FF / 8)) ? (u32)r8 * (FF * 2) + cu * 16u : 0x80000000u;
    asm volatile("" : "+v"(vf));
    const u64 epoch = ldg64(ws + WS_EPOCH);
    u32x4 Wb[16];
    const __amdgpu_buffer_rsrc_t rf_ = mkrsrc((const char*)(ch ? wu : wg) + (size_t)(128 * rq) * (FF * 2), 128 * FF * 2);
#pragma unroll
    for (int i = 0; i < 16; ++i) Wb[i] = ld16nt(rf_, vf, i * (8u * FF * 2));
    SB();
    __builtin_amdgcn_s_barrier();
    SB();
    float pf;
    {
      lds_wait(lds + L_CTL, 1);
      const u32x4 xa = *(const u32x4*)(lds + L_XT + r8 * 32 + rq * 8), xb = *(const u32x4*)(lds + L_XT + r8 * 32 + rq * 8 + 4);
      u32 xl[16];
#pragma unroll
      for (int i = 0; i < 8; ++i) { xl[i] = (i & 1) ? xa[i >> 1] >> 16 : xa[i >> 1] & 0xFFFFu; xl[8 + i] = (i & 1) ? xb[i >> 1] >> 16 : xb[i >> 1] & 0xFFFFu; }
      float a[8];
      gemv_rows(Wb, xl, a);
      pf = reduce_rows8(a, lane);
      asm volatile("" : "+v"(pf));
    }
    STAMPAT(1);
    u32x2 A[6], B[6];
    if (DEFER) lds_wait(lds + L_CTL + 2, 8);
    load_pairs<6>(mkrsrc(wd, FF * 1024), (48 * h + 2 * r) * 1024 + s * 128 + c * 8, A, B);
    SB();
    const u32 tag = (u32)epoch + 1u;
    STAMPAT(2);
    __syncthreads();
    STAMPAT(3);
    publish1(rws, WS_PF + g * 512 + lane * 8, pf, tag);
    u32x2 t[8];
#pragma unroll
    for (int k = 0; k < 8; ++k) t[k] = gather1(rws, WS_PF + h * 4096 + lane * 8, k * 512);
    SB();
    u32 Wd[6][4];
    pack_pairs<6>(A, B, Wd);
    SB();
    for (u32 spin = 0; spin < SPIN_CAP; ++spin) {
      bool any = false;
#pragma unroll
      for (int k = 0; k < 8; ++k) any |= t[k][1] != tag;
      if (!__any(any)) break;
      __builtin_amdgcn_s_sleep(PSL1);
      CFENCE();
#pragma unroll
      for (int k = 0; k < 8; ++k) { if (t[k][1] != tag) t[k] = gather1(rws, WS_PF + h * 4096 + lane * 8, k * 512); }
    }
    STAMPAT(4);
    const float gt = rbf(((asf(t[0][0]) + asf(t[2][0])) + asf(t[4][0])) + asf(t[6][0]));
    const float up = rbf(((asf(t[1][0]) + asf(t[3][0])) + asf(t[5][0])) + asf(t[7][0]));
    const float u = 0.7978845608028654f * (gt + 0.044715f * gt * gt * gt);
    const float th = 1.f - 2.f / (__expf(2.f * u) + 1.f);
    const int col = 8 * c8 + (lane >> 5) + 2 * ((lane >> 4) & 1) + 4 * ((lane >> 3) & 1);
    ((u16*)(lds + L_HS))[col] = (u16)pack2(rbf(0.5f * gt * (1.f + th)) * up, 0.f);
    CFENCE();
    const u32 hp = lds[L_HS + ((4 * c + r) & 31)];
    const float y = gemv_tail<6>(hp, Wd);
    if (H2PROD) publish1(rws, WS_O2 + g * 1024 + 512 + (4 * c + r) * 8, y, tag);
    else publish1(rws, WS_O2 + ((c + 16 * (r >> 1)) * 8 + s) * 1024 + h * 32 + (r & 1) * 16 + 8, y, tag);
    STAMPAT(5);
    STAMPFLUSH();
    return;
  }

  const int ocol = 64 * s + 4 * (h & 15) + 2 * (h >> 4);
  const u32x4 x4 = __builtin_amdgcn_raw_buffer_load_b128(mkrsrc(x, 1024), lane * 16, 0, XAUX);
  const u64 epoch = ldg64(ws + WS_EPOCH);
  const u32 xo = __builtin_amdgcn_raw_buffer_load_b32(mkrsrc(x, 1024), ocol * 2, 0, XAUX);
  u32x4 Wa[16];
  const int rq = s >> 1, ch = s & 1, r8 = lane >> 3, c8 = lane & 7;
  {
    const __amdgpu_buffer_rsrc_t rw = mkrsrc((const char*)wq + (size_t)(128 * rq) * 8192 + h * 256 + ch * 128, 127 * 8192 + 128);
    const u32 vw = r8 * 8192 + c8 * 16;
#pragma unroll
    for (int i = 0; i < 16; ++i) Wa[i] = ld16nt(rw, vw, i * 65536u);
  }
  if (lane < 4) lds[L_CTL + lane] = 0u;
  SB();
  asm volatile("s_waitcnt lgkmcnt(0)" ::: "memory");
  __builtin_amdgcn_s_barrier();
  SB();
  {
    float xf[8];
#pragma unroll
    for (int j = 0; j < 4; ++j) { xf[2 * j] = lo16(x4[j]); xf[2 * j + 1] = hi16(x4[j]); }
    float sum = 0.f;
#pragma unroll
    for (int j = 0; j < 8; ++j) sum += xf[j];
    const float mean = wavesum(sum) * (1.f / 512.f);
    float sq = 0.f;
#pragma unroll
    for (int j = 0; j < 8; ++j) sq = fmaf(xf[j] - mean, xf[j] - mean, sq);
    const float var = wavesum(sq) * (1.f / 512.f);
    const float mb = rbf(mean), rden = __builtin_amdgcn_rcpf(rbf(__builtin_amdgcn_sqrtf(rbf(rbf(var) + 1e-5f))));
#pragma unroll
    for (int j = 0; j < 4; ++j) {
      const u32 pr = pack2(rbf(xf[2 * j] - mb) * rden, rbf(xf[2 * j + 1] - mb) * rden);
      ((u16*)(lds + L_XT))[(2 * j) * 64 + lane] = (u16)pr; ((u16*)(lds + L_XT))[(2 * j + 1) * 64 + lane] = (u16)(pr >> 16);
    }
    lds_signal(lds + L_CTL, lane);
  }
  STAMPAT(1);
  const u32 tag = (u32)epoch + 1u;
  {
    const u32x4 xa = *(const u32x4*)(lds + L_XT + r8 * 32 + rq * 8), xb = *(const u32x4*)(lds + L_XT + r8 * 32 + rq * 8 + 4);
    u32 xl[16];
#pragma unroll
    for (int i = 0; i < 8; ++i) { xl[i] = (i & 1) ? xa[i >> 1] >> 16 : xa[i >> 1] & 0xFFFFu; xl[8 + i] = (i & 1) ? xb[i >> 1] >> 16 : xb[i >> 1] & 0xFFFFu; }
    float a[8];
    gemv_rows(Wa, xl, a);
    publish1(rws, WS_PQ + g * 512 + lane * 8, reduce_rows8(a, lane), tag);
  }
  STAMPAT(9);
  {
    u32x2 t[8];
#pragma unroll
    for (int k = 0; k < 8; ++k) t[k] = u32x2{0u, ~tag};
    __builtin_amdgcn_s_sleep(PSL00);
    for (u32 spin = 0; spin < SPIN_CAP; ++spin) {
      CFENCE();
#pragma unroll
      for (int k = 0; k < 8; ++k) { if (t[k][1] != tag) t[k] = gather1(rws, WS_PQ + h * 4096 + lane * 8, k * 512); }
      bool any = false;
#pragma unroll
      for (int k = 0; k < 8; ++k) any |= t[k][1] != tag;
      if (!__any(any)) break;
      __builtin_amdgcn_s_sleep(PSL0);
    }
    const float qa = ((asf(t[0][0]) + asf(t[2][0])) + asf(t[4][0])) + asf(t[6][0]);
    const float qb = ((asf(t[1][0]) + asf(t[3][0])) + asf(t[5][0])) + asf(t[7][0]);
    const int col = 8 * c8 + (lane >> 5) + 2 * ((lane >> 4) & 1) + 4 * ((lane >> 3) & 1);
    const u32 qp = pack2(qa, qb);
    ((u16*)(lds + L_QS))[col] = (u16)qp;
    ((u16*)(lds + L_QS))[64 + col] = (u16)(qp >> 16);
    lds_signal(lds + L_CTL + 1, lane);
  }
  STAMPAT(10);
  u32x2 C[16], Dd[16];
  if (DEFER) lds_wait(lds + L_CTL + 2, 8);
  load_pairs<16>(mkrsrc(wo, 4096 * 1024), (128 * h + 2 * r) * 1024 + s * 128 + c * 8, C, Dd);
  SB();
  STAMPAT(2);
  __syncthreads();
  STAMPAT(3);
  {
    const u32x2 t = *(const u32x2*)(lds + L_ML + (lane & 7) * 2);
    u32x2 po[8];
#pragma unroll
    for (int k = 0; k < 8; ++k) po[k] = *(const u32x2*)(lds + L_OP + k * 128 + lane * 2);
    const float M = max8(asf(t.x));
    const float f = __builtin_amdgcn_exp2f((asf(t.x) - M) * LOG2E);
    const float L = sum8(f * asf(t.y));
    float o0 = 0.f, o1 = 0.f;
#define MRG(I) { const float fb = dpp<BCAST + (I)>(f); o0 = fmaf(fb, asf(po[I].x), o0); o1 = fmaf(fb, asf(po[I].y), o1); }
    MRG(0) MRG(1) MRG(2) MRG(3) MRG(4) MRG(5) MRG(6) MRG(7)
#undef MRG
    publish2(rws, WS_O1 + g * 1024 + lane * 16, o0, o1, tag);
    if (lane == 0) publish2(rws, WS_ML + g * 16, M, L, tag);
  }
  SB();
  STAMPAT(4);
  u32 op;
  u32 Wo[16][4];
  {
    u32x4 t = gather2(rws, WS_ML + h * 128 + (lane & 7) * 16, 0), po[8];
#pragma unroll
    for (int k = 0; k < 8; ++k) po[k] = gather2(rws, WS_O1 + h * 8192 + lane * 16, k * 1024);
    SB();
    pack_pairs<16>(C, Dd, Wo);
    SB();
    STAMPAT(5);
    for (u32 spin = 0; spin < SPIN_CAP; ++spin) {
      bool any = bad2(t, tag);
#pragma unroll
      for (int k = 0; k < 8; ++k) any |= bad2(po[k], tag);
      if (!__any(any)) break;
      __builtin_amdgcn_s_sleep(PSL1);
      CFENCE();
      if (bad2(t, tag)) t = gather2(rws, WS_ML + h * 128 + (lane & 7) * 16, 0);
#pragma unroll
      for (int k = 0; k < 8; ++k) { if (bad2(po[k], tag)) po[k] = gather2(rws, WS_O1 + h * 8192 + lane * 16, k * 1024); }
    }
    const float mk = asf(t[0]), lk = asf(t[2]);
    const float M = max8(mk);
    const float f = __builtin_amdgcn_exp2f((mk - M) * LOG2E);
    const float L = sum8(f * lk);
    float o0 = 0.f, o1 = 0.f;
#define MRG(I) { const float fb = dpp<BCAST + (I)>(f); o0 = fmaf(fb, asf(po[I][0]), o0); o1 = fmaf(fb, asf(po[I][2]), o1); }
    MRG(0) MRG(1) MRG(2) MRG(3) MRG(4) MRG(5) MRG(6) MRG(7)
#undef MRG
    const float inv = 1.f / L;
    op = pack2(o0 * inv, o1 * inv);
  }
  STAMPAT(6);
  {
    const float sp = gemv_tail<16>(op, Wo);
    if (H2PROD) publish1(rws, WS_O2 + g * 1024 + (4 * c + r) * 8, sp, tag);
    else publish1(rws, WS_O2 + ((c + 16 * (r >> 1)) * 8 + s) * 1024 + h * 32 + (r & 1) * 16, sp, tag);
  }
  STAMPAT(7);
  {
    u32x4 t = {0u, ~tag, 0u, ~tag};
    const u32 p2 = H2PROD ? WS_O2 + ((lane & 31) * 8 + s) * 1024 + (lane >> 5) * 512 + (4 * (h & 15) + 2 * (h >> 4)) * 8 : WS_O2 + g * 1024 + lane * 16;
    for (u32 spin = 0; spin < SPIN_CAP; ++spin) {
      CFENCE();
      if (bad2(t, tag)) t = gather2(rws, p2, 0);
      if (!__any(bad2(t, tag))) break;
      __builtin_amdgcn_s_sleep(PSL2);
    }
    float res, other;
    if (H2PROD) {
      float v0 = rowsum(asf(t[0])), v1 = rowsum(asf(t[2]));
      v0 = swapsum16(v0, v0); v1 = swapsum16(v1, v1);
      auto s0 = __builtin_amdgcn_permlane32_swap(asu(v0), asu(v0), false, false);
      auto s1 = __builtin_amdgcn_permlane32_swap(asu(v1), asu(v1), false, false);
      const float sv = rbf((lane & 1) ? asf(s1[0]) : asf(s0[0])), yv = rbf((lane & 1) ? asf(s1[1]) : asf(s0[1]));
      const float gv = rbf(yv * __builtin_amdgcn_rcpf(1.f + __expf(-yv)));
      res = ((lane & 1) ? hi16(xo) : lo16(xo)) + rbf(sv * gv);
      other = dpp<XOR1>(res);
    } else {
      float sv = asf(t[0]), yv = asf(t[2]);
      sv += dpp<XOR2>(sv); yv += dpp<XOR2>(yv);
      sv += dpp<0x124>(sv); yv += dpp<0x124>(yv);
      sv += dpp<XOR8>(sv); yv += dpp<XOR8>(yv);
      sv = swapsum16(sv, sv); yv = swapsum16(yv, yv);
      sv = rbf(swapsum32(sv, sv)); yv = rbf(swapsum32(yv, yv));
      const float gv = rbf(yv * __builtin_amdgcn_rcpf(1.f + __expf(-yv)));
      res = ((lane & 1) ? hi16(xo) : lo16(xo)) + rbf(sv * gv);
      other = dpp<XOR1>(res);
    }
    if (lane == 0) {
      *(u32*)(out + ocol) = pack2(res, other);
      if (g == 0) stg64(ws + WS_EPOCH, epoch + 1);
    }
  }
  STAMPAT(8);
  STAMPFLUSH();
}

extern "C" void kernel_launch(void* const* d_in, const int* in_sizes, int n_in, void* d_out, int out_size, void* d_ws, size_t ws_size, hipStream_t stream) {
  fused_decode<<<256, 640, 0, stream>>>((const u16*)d_in[0], (const u16*)d_in[1], (const u16*)d_in[2], (const u16*)d_in[3], (const u16*)d_in[4],
                                        (const u16*)d_in[6], (const u16*)d_in[7], (const u16*)d_in[8], (u16*)d_out, (char*)d_ws);
}
```

```cpp
#include <hip/hip_runtime.h>
#include <hip/hip_bf16.h>
#include <stdint.h>

typedef unsigned short u16;
typedef unsigned u32;
typedef unsigned long long u64;
typedef u32 u32x2 __attribute__((ext_vector_type(2)));
typedef u32 u32x4 __attribute__((ext_vector_type(4)));
typedef __bf16 bf16x2 __attribute__((ext_vector_type(2)));
#define GAS __attribute__((address_space(1)))
#define LAS __attribute__((address_space(3)))
#define RLX_AGENT __ATOMIC_RELAXED, __HIP_MEMORY_SCOPE_AGENT
#define RLX_WG __ATOMIC_RELAXED, __HIP_MEMORY_SCOPE_WORKGROUP
#define CFENCE() asm volatile("" ::: "memory")
#define SB() __builtin_amdgcn_sched_barrier(0)

constexpr int DM = 512, NH = 32, DH = 128, SK = 32768, WIN = 4096, FF = 1376;
constexpr u32 HEADB = (u32)SK * DH * 2;
constexpr u32 WOFF = (u32)(SK - WIN) * DH * 2;
constexpr u32 SPIN_CAP = 1u << 18;
constexpr float LOG2E = 1.4426950408889634f;
constexpr u32 WS_EPOCH = 0, WS_O1 = 4096, WS_ML = WS_O1 + 256 * 1024, WS_PF = WS_ML + 256 * 16, WS_O2 = WS_PF + 256 * 512, WS_PQ = WS_O2 + 256 * 1024, WS_END = WS_PQ + 256 * 512;
constexpr u32 WS_STAMP = 2u << 20;
#define KPRE 2
#define KWIN 5
#define KTHR 16
#define XAUX 0
#define PSL0 8
#define PSL00 40
#define CODEPF 8
#define CODEBYTES 12288
#define H2PROD 1
#define SPLIT 10
#define DT 0
#define TAU 905
#define N0 24
#define PG 2
#define VTHR 0
#define NWO 0
#define NWD 0
#define DTRIG 8
#define BTRIG 8
#define ATRIG 7
#define DEFER 1
#define PSL1 1
#define PSL2 1

__device__ __forceinline__ float asf(u32 v) { return __builtin_bit_cast(float, v); }
__device__ __forceinline__ u32 asu(float v) { return __builtin_bit_cast(u32, v); }
__device__ __forceinline__ float dot2(u32 a, u32 b, float c) {
  return __builtin_amdgcn_fdot2_f32_bf16(__builtin_bit_cast(bf16x2, a), __builtin_bit_cast(bf16x2, b), c, false);
}
__device__ __forceinline__ u32 pack2(float lo, float hi) { bf16x2 r = {(__bf16)lo, (__bf16)hi}; return __builtin_bit_cast(u32, r); }
__device__ __forceinline__ float rbf(float v) { return (float)(__bf16)v; }
__device__ __forceinline__ float lo16(u32 v) { return asf(v << 16); }
__device__ __forceinline__ float hi16(u32 v) { return asf(v & 0xFFFF0000u); }
template <int CTRL> __device__ __forceinline__ u32 dppu(u32 x) { return (u32)__builtin_amdgcn_mov_dpp((int)x, CTRL, 0xf, 0xf, true); }
template <int CTRL> __device__ __forceinline__ float dpp(float x) { return asf(dppu<CTRL>(asu(x))); }
constexpr int XOR1 = 0xB1, XOR2 = 0x4E, XOR7 = 0x141, XOR15 = 0x140, XOR8 = 0x128, BCAST = 0x150;
__device__ __forceinline__ float swapsum16(float a, float b) {
  auto s = __builtin_amdgcn_permlane16_swap(asu(a), asu(b), false, false); return asf(s[0]) + asf(s[1]);
}
__device__ __forceinline__ float swapsum32(float a, float b) {
  auto s = __builtin_amdgcn_permlane32_swap(asu(a), asu(b), false, false); return asf(s[0]) + asf(s[1]);
}
__device__ __forceinline__ float swapmax16(float a) { auto s = __builtin_amdgcn_permlane16_swap(asu(a), asu(a), false, false); return fmaxf(asf(s[0]), asf(s[1])); }
__device__ __forceinline__ float swapmax32(float a) { auto s = __builtin_amdgcn_permlane32_swap(asu(a), asu(a), false, false); return fmaxf(asf(s[0]), asf(s[1])); }
__device__ __forceinline__ float rowsum(float v) { v += dpp<XOR1>(v); v += dpp<XOR2>(v); v += dpp<XOR7>(v); v += dpp<XOR15>(v); return v; }
__device__ __forceinline__ float rowmax(float v) { v = fmaxf(v, dpp<XOR1>(v)); v = fmaxf(v, dpp<XOR2>(v)); v = fmaxf(v, dpp<XOR7>(v)); v = fmaxf(v, dpp<XOR15>(v)); return v; }
__device__ __forceinline__ float wavesum(float v) { v = rowsum(v); v = swapsum16(v, v); return swapsum32(v, v); }
__device__ __forceinline__ float wavemax(float v) { v = rowmax(v); v = swapmax16(v); return swapmax32(v); }
__device__ __forceinline__ float sum8(float v) { v += dpp<XOR1>(v); v += dpp<XOR2>(v); v += dpp<XOR7>(v); return v; }
__device__ __forceinline__ float max8(float v) { v = fmaxf(v, dpp<XOR1>(v)); v = fmaxf(v, dpp<XOR2>(v)); v = fmaxf(v, dpp<XOR7>(v)); return v; }
__device__ __forceinline__ __amdgpu_buffer_rsrc_t mkrsrc(const void* p, u32 bytes) { return __builtin_amdgcn_make_buffer_rsrc((void*)p, 0, bytes, 0x00020000); }
__device__ __forceinline__ u32x4 ld16nt(__amdgpu_buffer_rsrc_t r, u32 voff, u32 soff) { return __builtin_amdgcn_raw_buffer_load_b128(r, voff, soff, 2); }
__device__ __forceinline__ u32x2 ld8nt(__amdgpu_buffer_rsrc_t r, u32 voff, u32 soff) { return __builtin_amdgcn_raw_buffer_load_b64(r, voff, soff, 2); }
__device__ __forceinline__ u64 ldg64(const char* p) { return __hip_atomic_load((GAS const u64*)p, RLX_AGENT); }
__device__ __forceinline__ void stg64(char* p, u64 v) { __hip_atomic_store((GAS u64*)p, v, RLX_AGENT); }
__device__ __forceinline__ void publish2(__amdgpu_buffer_rsrc_t r, u32 off, float a, float b, u32 tag) {
  __builtin_amdgcn_raw_buffer_store_b128(u32x4{asu(a), tag, asu(b), tag}, r, off, 0, 16);
}
__device__ __forceinline__ void publish1(__amdgpu_buffer_rsrc_t r, u32 off, float a, u32 tag) {
  __builtin_amdgcn_raw_buffer_store_b64(u32x2{asu(a), tag}, r, off, 0, 16);
}
__device__ __forceinline__ u32x4 gather2(__amdgpu_buffer_rsrc_t r, u32 voff, u32 soff) { return __builtin_amdgcn_raw_buffer_load_b128(r, voff, soff, 16); }
__device__ __forceinline__ u32x2 gather1(__amdgpu_buffer_rsrc_t r, u32 voff, u32 soff) { return __builtin_amdgcn_raw_buffer_load_b64(r, voff, soff, 16); }
__device__ __forceinline__ bool bad2(u32x4 v, u32 tag) { return ((v[1] ^ tag) | (v[3] ^ tag)) != 0u; }
__device__ __forceinline__ void lds_wait(u32* p, u32 want) {
  for (u32 spin = 0; __hip_atomic_load((LAS u32*)p, RLX_WG) < want && spin < SPIN_CAP * 16; ++spin) __builtin_amdgcn_s_sleep(1);
  CFENCE();
}
__device__ __forceinline__ void lds_wait0(u32* p, u32 want) {
  for (u32 spin = 0; __hip_atomic_load((LAS u32*)p, RLX_WG) < want && spin < SPIN_CAP * 64; ++spin) {}
  CFENCE();
}
__device__ __forceinline__ void lds_signal(u32* p, int lane) {
  CFENCE();
  if (lane == 0) __hip_atomic_fetch_add((LAS u32*)p, 1u, RLX_WG);
  CFENCE();
}
#define STAMPAT(k) do {} while (0)
#define STAMPDECL do {} while (0)
#define STAMPFLUSH() do {} while (0)

template <int N> __device__ __forceinline__ float gemv_tail(u32 pairs, const u32 (&Wp)[N][4]) {
  float a0 = 0.f, a1 = 0.f, a2 = 0.f, a3 = 0.f;
#define GSTEP(I) if constexpr ((I) < N) { const u32 pb = dppu<BCAST + (I)>(pairs); a0 = dot2(pb, Wp[I][0], a0); a1 = dot2(pb, Wp[I][1], a1); a2 = dot2(pb, Wp[I][2], a2); a3 = dot2(pb, Wp[I][3], a3); }
  GSTEP(0) GSTEP(1) GSTEP(2) GSTEP(3) GSTEP(4) GSTEP(5) GSTEP(6) GSTEP(7) GSTEP(8) GSTEP(9) GSTEP(10) GSTEP(11) GSTEP(12) GSTEP(13) GSTEP(14) GSTEP(15)
#undef GSTEP
  const float b0 = swapsum32(a0, a2), b1 = swapsum32(a1, a3);
  return swapsum16(b0, b1);
}
template <int N> __device__ __forceinline__ void load_pairs(__amdgpu_buffer_rsrc_t rs, u32 voff, u32x2 (&A)[N], u32x2 (&B)[N]) {
#pragma unroll
  for (int i = 0; i < N; ++i) { A[i] = ld8nt(rs, voff + i * 8192u, 0); B[i] = ld8nt(rs, voff + i * 8192u + 1024u, 0); }
}
template <int N> __device__ __forceinline__ void pack_pairs(const u32x2 (&A)[N], const u32x2 (&B)[N], u32 (&Wp)[N][4]) {
#pragma unroll
  for (int i = 0; i < N; ++i) {
    Wp[i][0] = __builtin_amdgcn_perm(B[i].x, A[i].x, 0x05040100u); Wp[i][1] = __builtin_amdgcn_perm(B[i].x, A[i].x, 0x07060302u);
    Wp[i][2] = __builtin_amdgcn_perm(B[i].y, A[i].y, 0x05040100u); Wp[i][3] = __builtin_amdgcn_perm(B[i].y, A[i].y, 0x07060302u);
    asm volatile("" : "+v"(Wp[i][0]), "+v"(Wp[i][1]), "+v"(Wp[i][2]), "+v"(Wp[i][3]));
  }
}
__device__ __forceinline__ void gemv_rows(const u32x4 (&Wt)[16], const u32 (&xl)[16], float (&a)[8]) {
#pragma unroll
  for (int j = 0; j < 8; ++j) a[j] = 0.f;
#pragma unroll
  for (int i = 0; i < 16; ++i) {
    const u32 xh = xl[i] << 16;
#pragma unroll
    for (int j = 0; j < 4; ++j) { a[2 * j] = dot2(Wt[i][j], xl[i], a[2 * j]); a[2 * j + 1] = dot2(Wt[i][j], xh, a[2 * j + 1]); }
  }
}
__device__ __forceinline__ void reduce_rows(const float (&a)[8], float& o0, float& o1) {
  const float b0 = swapsum32(a[0], a[4]), b1 = swapsum32(a[1], a[5]), b2 = swapsum32(a[2], a[6]), b3 = swapsum32(a[3], a[7]);
  o0 = swapsum16(b0, b2); o1 = swapsum16(b1, b3);
}

__device__ __forceinline__ float reduce_rows8(const float (&a)[8], int lane) {
  float bq[4];
#pragma unroll
  for (int j = 0; j < 4; ++j) { const float t0 = a[j] + dpp<XOR8>(a[j]), t1 = a[j + 4] + dpp<XOR8>(a[j + 4]); bq[j] = (lane & 8) ? t1 : t0; }
  const float c0 = swapsum16(bq[0], bq[2]), c1 = swapsum16(bq[1], bq[3]);
  return swapsum32(c0, c1);
}

constexpr int L_CTL = 0;
constexpr int L_XT = 4;
constexpr int L_QS = L_XT + 256;
constexpr int L_OP = L_QS + 64;
constexpr int L_ML = L_OP + 1024;
constexpr int L_HS = L_ML + 16;
constexpr int L_END = L_HS + 32;

__global__ void __launch_bounds__(640, 3) fused_decode(const u16* __restrict__ x, const u16* __restrict__ kc, const u16* __restrict__ vc,
                                                       const u16* __restrict__ wq, const u16* __restrict__ wo, const u16* __restrict__ wg,
                                                       const u16* __restrict__ wu, const u16* __restrict__ wd, u16* out, char* ws) {
  __shared__ __attribute__((aligned(16))) u32 lds[L_END];
  const int b = blockIdx.x, idx = b >> 3;
  const int h = (b & 7) * 4 + (idx >> 3), s = idx & 7, g = h * 8 + s;
  const int lane = threadIdx.x & 63;
  const int wave = __builtin_amdgcn_readfirstlane(threadIdx.x >> 6);
  const int r = lane >> 4, c = lane & 15;
  STAMPDECL;
  STAMPAT(0);
  u32 cpf0 = 0, cpf1 = 0;
  if (CODEPF && wave == 0 && blockIdx.x < CODEPF) {
    u64 pc;
    asm volatile("s_getpc_b64 %0" : "=s"(pc));
    const GAS char* cb = (const GAS char*)(pc & ~127ull);
    cpf0 = *(const GAS u32*)(cb + lane * 128);
    if (lane < (CODEBYTES - 8192) / 128) cpf1 = *(const GAS u32*)(cb + 8192 + lane * 128);
  }
  if (threadIdx.x < 4) lds[L_CTL + threadIdx.x] = 0u;
  asm volatile("" :: "s"(x), "s"(kc), "s"(vc), "s"(wq), "s"(wo), "s"(wg), "s"(wu), "s"(wd), "s"(out), "s"(ws));
  asm volatile("s_waitcnt lgkmcnt(0)" ::: "memory");
  __builtin_amdgcn_s_barrier();
  SB();
  const u64 T0 = __builtin_amdgcn_s_memrealtime();
  const u32 tau = (u32)TAU - (u32)idx * (u32)DT;
  auto pace = [&](u32 n) { if (TAU) { const u64 until = T0 + (u64)((n * tau) >> 8); while (__builtin_amdgcn_s_memrealtime() < until) __builtin_amdgcn_s_sleep(1); } };

  if (wave >= 2) {
    const int w = wave - 2;
    const size_t base = (size_t)h * HEADB + WOFF + (u32)s * 131072u + (u32)w * 16384u;
    const __amdgpu_buffer_rsrc_t rk = mkrsrc((const char*)kc + base, 16384), rv = mkrsrc((const char*)vc + base, 16384);
    const u32 voff = lane * 16, rot = idx + 4 * w;
    u32x4 K[16], V[16];
#pragma unroll
    for (int i = 0; i < 16; ++i) {
      if (i % PG == 0) { SB(); pace(N0 + 8 * i + PG * w); SB(); }
      K[i] = ld16nt(rk, voff, ((i + rot) & 15) * 1024u);
      if (i >= KWIN - 1 && i < KTHR) { SB(); asm volatile("s_waitcnt vmcnt(%0)" :: "i"(KWIN - 1) : "memory"); SB(); }
    }
#define VISSUE(I) { if ((I) % PG == 0) { SB(); pace(N0 + 128 + 8 * (I) + PG * w); SB(); } V[I] = ld16nt(rv, voff, (((I) + rot) & 15) * 1024u); SB(); }
#pragma unroll
    for (int i = 0; i < SPLIT; ++i) VISSUE(i)
    STAMPAT(3);
    lds_wait(lds + L_CTL + 1, 1);
    const u32x4 q4 = *(const u32x4*)(lds + L_QS + 4 * c);
    STAMPAT(4);
    float xsc[16];
#pragma unroll
    for (int i = 0; i < 16; ++i) xsc[i] = dot2(K[i][3], q4[3], dot2(K[i][2], q4[2], dot2(K[i][1], q4[1], dot2(K[i][0], q4[0], 0.f))));
#define TR_STEP(HALF, CTRL, BIT) _Pragma("unroll") for (int k = 0; k < (HALF); ++k) { const float t0 = xsc[k] + dpp<CTRL>(xsc[k]), t1 = xsc[k + (HALF)] + dpp<CTRL>(xsc[k + (HALF)]); xsc[k] = (c & (BIT)) ? t1 : t0; }
    TR_STEP(8, XOR8, 8) TR_STEP(4, XOR7, 4) TR_STEP(2, XOR2, 2) TR_STEP(1, XOR1, 1)
#undef TR_STEP
    const float sc = rbf(rbf(xsc[0]) * 0.08838834764831845f);
    const float m = wavemax(sc);
    const float p = __builtin_amdgcn_exp2f((sc - m) * LOG2E);
    const u32 pl = pack2(p, 0.f), ph = pl << 16;
    const float l = wavesum(p);
    STAMPAT(5);
    float a[8];
#pragma unroll
    for (int j = 0; j < 8; ++j) a[j] = 0.f;
#define PV(I) { const u32 bl = dppu<BCAST + (I)>(pl), bh = dppu<BCAST + (I)>(ph); _Pragma("unroll") for (int j = 0; j < 4; ++j) { a[2 * j] = dot2(V[I][j], bl, a[2 * j]); a[2 * j + 1] = dot2(V[I][j], bh, a[2 * j + 1]); } }
    VISSUE(10) VISSUE(11) PV(0) PV(1) PV(2) PV(3) PV(4) PV(5) SB();
    VISSUE(12) VISSUE(13) PV(6) PV(7) PV(8) PV(9) SB();
    VISSUE(14) VISSUE(15)
    lds_signal(lds + L_CTL + 2, lane);
    PV(10) PV(11) PV(12) PV(13) PV(14) PV(15)
#undef PV
#undef VISSUE
    float o0, o1;
    reduce_rows(a, o0, o1);
    *(u32x2*)(lds + L_OP + w * 128 + lane * 2) = u32x2{asu(o0), asu(o1)};
    if (lane == 0) *(u32x2*)(lds + L_ML + w * 2) = u32x2{asu(m), asu(l)};
    lds_signal(lds + L_CTL + 3, lane);
    STAMPAT(6);
    STAMPFLUSH();
    return;
  }

  __builtin_amdgcn_s_setprio(3);
  const __amdgpu_buffer_rsrc_t rws = mkrsrc(ws, WS_END);
  if (wave == 1) {
    const int rq = s >> 1, ch = s & 1, r8 = lane >> 3, c8 = lane & 7;
    const u32 cu = (u32)__builtin_amdgcn_readfirstlane(6 * h) + (u32)c8;
    u32 vf = ((c8 < 6) & (cu < FF / 8)) ? (u32)r8 * (FF * 2) + cu * 16u : 0x80000000u;
    asm volatile("" : "+v"(vf));
    const u64 epoch = ldg64(ws + WS_EPOCH);
    u32x4 Wb[16];
    const __amdgpu_buffer_rsrc_t rf_ = mkrsrc((const char*)(ch ? wu : wg) + (size_t)(128 * rq) * (FF * 2), 128 * FF * 2);
#pragma unroll
    for (int i = 0; i < 16; ++i) Wb[i] = ld16nt(rf_, vf, i * (8u * FF * 2));
    SB();
    float pf;
    {
      lds_wait(lds + L_CTL, 1);
      const u32x4 xa = *(const u32x4*)(lds + L_XT + r8 * 32 + rq * 8), xb = *(const u32x4*)(lds + L_XT + r8 * 32 + rq * 8 + 4);
      u32 xl[16];
#pragma unroll
      for (int i = 0; i < 8; ++i) { xl[i] = (i & 1) ? xa[i >> 1] >> 16 : xa[i >> 1] & 0xFFFFu; xl[8 + i] = (i & 1) ? xb[i >> 1] >> 16 : xb[i >> 1] & 0xFFFFu; }
      float a[8];
      gemv_rows(Wb, xl, a);
      pf = reduce_rows8(a, lane);
      asm volatile("" : "+v"(pf));
    }
    const u32 tag = (u32)epoch + 1u;
    publish1(rws, WS_PF + g * 512 + lane * 8, pf, tag);
    STAMPAT(1);
    lds_wait(lds + L_CTL + 1, 1);
    u32 hp;
    {
      u32x2 t[8];
#pragma unroll
      for (int k = 0; k < 8; ++k) t[k] = u32x2{0u, ~tag};
      for (u32 spin = 0; spin < SPIN_CAP; ++spin) {
        CFENCE();
#pragma unroll
        for (int k = 0; k < 8; ++k) { if (t[k][1] != tag) t[k] = gather1(rws, WS_PF + h * 4096 + lane * 8, k * 512); }
        bool any = false;
#pragma unroll
        for (int k = 0; k < 8; ++k) any |= t[k][1] != tag;
        if (!__any(any)) break;
        __builtin_amdgcn_s_sleep(PSL0);
      }
      const float gt = rbf(((asf(t[0][0]) + asf(t[2][0])) + asf(t[4][0])) + asf(t[6][0]));
      const float up = rbf(((asf(t[1][0]) + asf(t[3][0])) + asf(t[5][0])) + asf(t[7][0]));
      const float u = 0.7978845608028654f * (gt + 0.044715f * gt * gt * gt);
      const float th = 1.f - 2.f * __builtin_amdgcn_rcpf(__expf(2.f * u) + 1.f);
      const int col = 8 * c8 + (lane >> 5) + 2 * ((lane >> 4) & 1) + 4 * ((lane >> 3) & 1);
      ((u16*)(lds + L_HS))[col] = (u16)pack2(rbf(0.5f * gt * (1.f + th)) * up, 0.f);
      CFENCE();
      hp = lds[L_HS + ((4 * c + r) & 31)];
    }
    STAMPAT(2);
    u32x2 A[6], B[6];
    if (DEFER == 1) { lds_wait(lds + L_CTL + 2, DTRIG); pace(N0 + 256 + NWD); }
    if (DEFER >= 2) lds_wait(lds + L_CTL + 3, BTRIG);
    load_pairs<6>(mkrsrc(wd, FF * 1024), (48 * h + 2 * r) * 1024 + s * 128 + c * 8, A, B);
    STAMPAT(3);
    u32 Wd[6][4];
    pack_pairs<6>(A, B, Wd);
    const float y = gemv_tail<6>(hp, Wd);
    if (H2PROD) publish1(rws, WS_O2 + g * 1024 + 512 + (4 * c + r) * 8, y, tag);
    else publish1(rws, WS_O2 + ((c + 16 * (r >> 1)) * 8 + s) * 1024 + h * 32 + (r & 1) * 16 + 8, y, tag);
    STAMPAT(5);
    STAMPFLUSH();
    return;
  }

  const int ocol = 64 * s + 4 * (h & 15) + 2 * (h >> 4);
  const u32x4 x4 = __builtin_amdgcn_raw_buffer_load_b128(mkrsrc(x, 1024), lane * 16, 0, XAUX);
  const u64 epoch = ldg64(ws + WS_EPOCH);
  const u32 xo = __builtin_amdgcn_raw_buffer_load_b32(mkrsrc(x, 1024), ocol * 2, 0, XAUX);
  u32x4 Wa[16];
  const int rq = s >> 1, ch = s & 1, r8 = lane >> 3, c8 = lane & 7;
  {
    const __amdgpu_buffer_rsrc_t rw = mkrsrc((const char*)wq + (size_t)(128 * rq) * 8192 + h * 256 + ch * 128, 127 * 8192 + 128);
    const u32 vw = r8 * 8192 + c8 * 16;
#pragma unroll
    for (int i = 0; i < 16; ++i) Wa[i] = ld16nt(rw, vw, i * 65536u);
  }
  SB();
  STAMPAT(15);
  {
    float xf[8];
#pragma unroll
    for (int j = 0; j < 4; ++j) { xf[2 * j] = lo16(x4[j]); xf[2 * j + 1] = hi16(x4[j]); }
    float sum = 0.f;
#pragma unroll
    for (int j = 0; j < 8; ++j) sum += xf[j];
    const float mean = wavesum(sum) * (1.f / 512.f);
    float sq = 0.f;
#pragma unroll
    for (int j = 0; j < 8; ++j) sq = fmaf(xf[j] - mean, xf[j] - mean, sq);
    const float var = wavesum(sq) * (1.f / 512.f);
    const float mb = rbf(mean), rden = __builtin_amdgcn_rcpf(rbf(__builtin_amdgcn_sqrtf(rbf(rbf(var) + 1e-5f))));
#pragma unroll
    for (int j = 0; j < 4; ++j) {
      const u32 pr = pack2(rbf(xf[2 * j] - mb) * rden, rbf(xf[2 * j + 1] - mb) * rden);
      ((u16*)(lds + L_XT))[(2 * j) * 64 + lane] = (u16)pr; ((u16*)(lds + L_XT))[(2 * j + 1) * 64 + lane] = (u16)(pr >> 16);
    }
    lds_signal(lds + L_CTL, lane);
  }
  asm volatile("" :: "v"(cpf0), "v"(cpf1));
  STAMPAT(1);
  const u32 tag = (u32)epoch + 1u;
  {
    const u32x4 xa = *(const u32x4*)(lds + L_XT + r8 * 32 + rq * 8), xb = *(const u32x4*)(lds + L_XT + r8 * 32 + rq * 8 + 4);
    u32 xl[16];
#pragma unroll
    for (int i = 0; i < 8; ++i) { xl[i] = (i & 1) ? xa[i >> 1] >> 16 : xa[i >> 1] & 0xFFFFu; xl[8 + i] = (i & 1) ? xb[i >> 1] >> 16 : xb[i >> 1] & 0xFFFFu; }
    float a[8];
    gemv_rows(Wa, xl, a);
    publish1(rws, WS_PQ + g * 512 + lane * 8, reduce_rows8(a, lane), tag);
  }
  STAMPAT(9);
  {
    u32x2 t[8];
#pragma unroll
    for (int k = 0; k < 8; ++k) t[k] = u32x2{0u, ~tag};
    __builtin_amdgcn_s_sleep(PSL00);
    for (u32 spin = 0; spin < SPIN_CAP; ++spin) {
      CFENCE();
#pragma unroll
      for (int k = 0; k < 8; ++k) { if (t[k][1] != tag) t[k] = gather1(rws, WS_PQ + h * 4096 + lane * 8, k * 512); }
      bool any = false;
#pragma unroll
      for (int k = 0; k < 8; ++k) any |= t[k][1] != tag;
      if (!__any(any)) break;
      __builtin_amdgcn_s_sleep(PSL0);
    }
    const float qa = ((asf(t[0][0]) + asf(t[2][0])) + asf(t[4][0])) + asf(t[6][0]);
    const float qb = ((asf(t[1][0]) + asf(t[3][0])) + asf(t[5][0])) + asf(t[7][0]);
    const int col = 8 * c8 + (lane >> 5) + 2 * ((lane >> 4) & 1) + 4 * ((lane >> 3) & 1);
    const u32 qp = pack2(qa, qb);
    ((u16*)(lds + L_QS))[col] = (u16)qp;
    ((u16*)(lds + L_QS))[64 + col] = (u16)(qp >> 16);
    lds_signal(lds + L_CTL + 1, lane);
  }
  STAMPAT(10);
  u32x2 C[16], Dd[16];
  if (DEFER == 1) { lds_wait(lds + L_CTL + 2, DTRIG); pace(N0 + 256 + NWO); }
  if (DEFER == 3) lds_wait0(lds + L_CTL + 3, ATRIG);
  if (DEFER != 2) load_pairs<16>(mkrsrc(wo, 4096 * 1024), (128 * h + 2 * r) * 1024 + s * 128 + c * 8, C, Dd);
  SB();
  STAMPAT(2);
  lds_wait0(lds + L_CTL + 3, 8);
  STAMPAT(3);
  {
    const u32x2 t = *(const u32x2*)(lds + L_ML + (lane & 7) * 2);
    u32x2 po[8];
#pragma unroll
    for (int k = 0; k < 8; ++k) po[k] = *(const u32x2*)(lds + L_OP + k * 128 + lane * 2);
    const float M = max8(asf(t.x));
    const float f = __builtin_amdgcn_exp2f((asf(t.x) - M) * LOG2E);
    const float L = sum8(f * asf(t.y));
    float o0 = 0.f, o1 = 0.f;
#define MRG(I) { const float fb = dpp<BCAST + (I)>(f); o0 = fmaf(fb, asf(po[I].x), o0); o1 = fmaf(fb, asf(po[I].y), o1); }
    MRG(0) MRG(1) MRG(2) MRG(3) MRG(4) MRG(5) MRG(6) MRG(7)
#undef MRG
    publish2(rws, WS_O1 + g * 1024 + lane * 16, o0, o1, tag);
    if (lane == 0) publish2(rws, WS_ML + g * 16, M, L, tag);
  }
  SB();
  STAMPAT(4);
  u32 op;
  u32 Wo[16][4];
  {
    u32x4 t = gather2(rws, WS_ML + h * 128 + (lane & 7) * 16, 0), po[8];
#pragma unroll
    for (int k = 0; k < 8; ++k) po[k] = gather2(rws, WS_O1 + h * 8192 + lane * 16, k * 1024);
    SB();
    if (DEFER == 2) { load_pairs<16>(mkrsrc(wo, 4096 * 1024), (128 * h + 2 * r) * 1024 + s * 128 + c * 8, C, Dd); SB(); }
    pack_pairs<16>(C, Dd, Wo);
    SB();
    STAMPAT(5);
    for (u32 spin = 0; spin < SPIN_CAP; ++spin) {
      bool any = bad2(t, tag);
#pragma unroll
      for (int k = 0; k < 8; ++k) any |= bad2(po[k], tag);
      if (!__any(any)) break;
      __builtin_amdgcn_s_sleep(PSL1);
      CFENCE();
      if (bad2(t, tag)) t = gather2(rws, WS_ML + h * 128 + (lane & 7) * 16, 0);
#pragma unroll
      for (int k = 0; k < 8; ++k) { if (bad2(po[k], tag)) po[k] = gather2(rws, WS_O1 + h * 8192 + lane * 16, k * 1024); }
    }
    const float mk = asf(t[0]), lk = asf(t[2]);
    const float M = max8(mk);
    const float f = __builtin_amdgcn_exp2f((mk - M) * LOG2E);
    const float L = sum8(f * lk);
    float o0 = 0.f, o1 = 0.f;
#define MRG(I) { const float fb = dpp<BCAST + (I)>(f); o0 = fmaf(fb, asf(po[I][0]), o0); o1 = fmaf(fb, asf(po[I][2]), o1); }
    MRG(0) MRG(1) MRG(2) MRG(3) MRG(4) MRG(5) MRG(6) MRG(7)
#undef MRG
    const float inv = 1.f / L;
    op = pack2(o0 * inv, o1 * inv);
  }
  STAMPAT(6);
  {
    const float sp = gemv_tail<16>(op, Wo);
    if (H2PROD) publish1(rws, WS_O2 + g * 1024 + (4 * c + r) * 8, sp, tag);
    else publish1(rws, WS_O2 + ((c + 16 * (r >> 1)) * 8 + s) * 1024 + h * 32 + (r & 1) * 16, sp, tag);
  }
  STAMPAT(7);
  {
    u32x4 t = {0u, ~tag, 0u, ~tag};
    const u32 p2 = H2PROD ? WS_O2 + ((lane & 31) * 8 + s) * 1024 + (lane >> 5) * 512 + (4 * (h & 15) + 2 * (h >> 4)) * 8 : WS_O2 + g * 1024 + lane * 16;
    for (u32 spin = 0; spin < SPIN_CAP; ++spin) {
      CFENCE();
      if (bad2(t, tag)) t = gather2(rws, p2, 0);
      if (!__any(bad2(t, tag))) break;
      __builtin_amdgcn_s_sleep(PSL2);
    }
    float res, other;
    if (H2PROD) {
      float v0 = rowsum(asf(t[0])), v1 = rowsum(asf(t[2]));
      v0 = swapsum16(v0, v0); v1 = swapsum16(v1, v1);
      auto s0 = __builtin_amdgcn_permlane32_swap(asu(v0), asu(v0), false, false);
      auto s1 = __builtin_amdgcn_permlane32_swap(asu(v1), asu(v1), false, false);
      const float sv = rbf((lane & 1) ? asf(s1[0]) : asf(s0[0])), yv = rbf((lane & 1) ? asf(s1[1]) : asf(s0[1]));
      const float gv = rbf(yv * __builtin_amdgcn_rcpf(1.f + __expf(-yv)));
      res = ((lane & 1) ? hi16(xo) : lo16(xo)) + rbf(sv * gv);
      other = dpp<XOR1>(res);
    } else {
      float sv = asf(t[0]), yv = asf(t[2]);
      sv += dpp<XOR2>(sv); yv += dpp<XOR2>(yv);
      sv += dpp<0x124>(sv); yv += dpp<0x124>(yv);
      sv += dpp<XOR8>(sv); yv += dpp<XOR8>(yv);
      sv = swapsum16(sv, sv); yv = swapsum16(yv, yv);
      sv = rbf(swapsum32(sv, sv)); yv = rbf(swapsum32(yv, yv));
      const float gv = rbf(yv * __builtin_amdgcn_rcpf(1.f + __expf(-yv)));
      res = ((lane & 1) ? hi16(xo) : lo16(xo)) + rbf(sv * gv);
      other = dpp<XOR1>(res);
    }
    if (lane == 0) {
      *(u32*)(out + ocol) = pack2(res, other);
      if (g == 0) stg64(ws + WS_EPOCH, epoch + 1);
    }
  }
  STAMPAT(8);
  STAMPFLUSH();
}

extern "C" void kernel_launch(void* const* d_in, const int* in_sizes, int n_in, void* d_out, int out_size, void* d_ws, size_t ws_size, hipStream_t stream) {
  fused_decode<<<256, 640, 0, stream>>>((const u16*)d_in[0], (const u16*)d_in[1], (const u16*)d_in[2], (const u16*)d_in[3], (const u16*)d_in[4],
                                        (const u16*)d_in[6], (const u16*)d_in[7], (const u16*)d_in[8], (u16*)d_out, (char*)d_ws);
}
```
